```python
import torch

def _softmax(x, axis=-1):
    return torch.softmax(x, dim=axis)
def _gelu(x):
    return torch.nn.functional.gelu(x, approximate="tanh")
def _silu(x):
    return torch.nn.functional.silu(x)
def _sigmoid(x):
    return torch.sigmoid(x)
def _layernorm(x, axis=-1, eps=1e-5):
    m = x.mean(dim=axis, keepdim=True)
    v = x.var(dim=axis, keepdim=True, unbiased=False)
    return (x - m) / torch.sqrt(v + eps)
def _scatter_add(tgt, idx, src, axis=0):
    return tgt.clone().index_add_(axis, idx, src)
def _segment_sum(src, seg, n):
    out = torch.zeros((n,) + src.shape[1:], dtype=src.dtype)
    return out.index_add_(0, seg, src)
def _pad(x, pw):
    flat = tuple(v for pair in reversed(pw) for v in pair)
    return torch.nn.functional.pad(x, flat)

kernel_name = 'standalone__standard_2d_1d_3d_conv__vision'

def setup_inputs():

    _w = (lambda a: (lambda t: t.to(torch.bfloat16) if t.is_floating_point() else t)(torch.as_tensor(a)))
    H = W = 384
    C = 96
    K = 3
    return {
        "x": _w((lambda *s: torch.randn(*s, dtype=torch.bfloat16) * 0.05)(1, H, W, C)),
        "w": _w((lambda *s: torch.randn(*s, dtype=torch.bfloat16) * 0.05)(K, K, C, C)),
        "b": _w((lambda *s: torch.randn(*s, dtype=torch.bfloat16) * 0.05)(C)),
    }

def reference(x, w, b):
    B, H, W, Cin = x.shape
    K = w.shape[0]
    pl = (K - 1) // 2
    pr = K // 2
    xp = _pad(x, ((0, 0), (pl, pr), (pl, pr), (0, 0)))
    out = sum(
        torch.einsum('bhwc,cd->bhwd', xp[:, i:i+H, j:j+W, :], w[i, j])
        for i in range(K) for j in range(K)
    )
    return out + b[None, None, None, :]
```

```cpp
#include <hip/hip_runtime.h>
#include <hip/hip_bf16.h>
typedef short bf16x8 __attribute__((ext_vector_type(8)));
typedef float f32x4 __attribute__((ext_vector_type(4)));
constexpr int H=384,W=384,C=96;
__global__ __launch_bounds__(256) void conv_simple(const unsigned short* __restrict__ x, const unsigned short* __restrict__ w,
                            const unsigned short* __restrict__ b, unsigned short* __restrict__ out) {
  int wave = (blockIdx.x * 256 + threadIdx.x) >> 6;
  int lane = threadIdx.x & 63;
  int p = lane & 15, g = lane >> 4;
  int y = wave / (W/16), x0 = (wave % (W/16)) * 16;
  f32x4 acc[6];
  for (int t = 0; t < 6; ++t) for (int j = 0; j < 4; ++j) acc[t][j] = __uint_as_float((unsigned)b[t*16 + g*4 + j] << 16);
  for (int dy = 0; dy < 3; ++dy) for (int dx = 0; dx < 3; ++dx) {
    int yy = y + dy - 1, xx = x0 + p + dx - 1;
    bool ok = yy >= 0 && yy < H && xx >= 0 && xx < W;
    for (int s = 0; s < 3; ++s) {
      bf16x8 xf = {0,0,0,0,0,0,0,0};
      if (ok) xf = *(const bf16x8*)(x + ((long)yy * W + xx) * C + s*32 + g*8);
      for (int t = 0; t < 6; ++t) {
        bf16x8 wf;
        for (int j = 0; j < 8; ++j) wf[j] = w[(((dy*3+dx)*C) + s*32 + g*8 + j) * C + t*16 + p];
        acc[t] = __builtin_amdgcn_mfma_f32_16x16x32_bf16(wf, xf, acc[t], 0,0,0);
      }
    }
  }
  for (int t = 0; t < 6; ++t) for (int j = 0; j < 4; ++j) {
    __hip_bfloat16 v = __float2bfloat16(acc[t][j]);
    out[((long)y*W + x0 + p) * C + t*16 + g*4 + j] = *(unsigned short*)&v;
  }
}
extern "C" void kernel_launch(void* const* d_in, const int* in_sizes, int n_in,
                              void* d_out, int out_size, void* d_ws, size_t ws_size,
                              hipStream_t stream) {
  conv_simple<<<H*W/16/4, 256, 0, stream>>>((const unsigned short*)d_in[0], (const unsigned short*)d_in[1], (const unsigned short*)d_in[2], (unsigned short*)d_out);
}
```

```cpp
#include <hip/hip_runtime.h>
__global__ __launch_bounds__(256, 1) __attribute__((aligned(4096)))
void conv3x3_kernel(const void* x, const void* w, const void* b, void* out) {
  __shared__ char lds[163840];
  const void* ka = (const void*)__builtin_amdgcn_kernarg_segment_ptr();
  int wave = __builtin_amdgcn_readfirstlane(threadIdx.x >> 6);
  int bid = blockIdx.x;
  asm volatile(
"s_load_dwordx8 s[8:15], %0, 0x0\n"
"s_getpc_b64 s[56:57]\n"
".Lconv_pc:\n"
"v_mbcnt_lo_u32_b32 v96, -1, 0\n"
"s_lshr_b32 s49, %2, 3\n"
"v_mbcnt_hi_u32_b32 v96, -1, v96\n"
"s_lshl_b32 s58, s49, 2\n"
"v_lshlrev_b32 v99, 4, v96\n"
"s_add_u32 s58, s58, %1\n"
"s_and_b32 s30, s58, 31\n"
"s_lshl_b32 s30, s30, 10\n"
"s_min_u32 s30, s30, .Lconv_end-.Lconv_pc-1024\n"
"s_add_u32 s56, s56, s30\n"
"s_addc_u32 s57, s57, 0\n"
"global_load_dwordx4 v[236:239], v99, s[56:57]\n"
"s_and_b32 s48, %2, 7\n"
"s_lshl_b32 s48, s48, 3\n"
"s_lshr_b32 s50, s49, 2\n"
"s_add_u32 s48, s48, s50\n"
"s_and_b32 s50, s49, 3\n"
"s_lshl_b32 s44, %1, 12\n"
"s_lshl_b32 s45, %1, 10\n"
"s_mul_i32 s46, %1, 3072\n"
"s_bitcmp0_b32 s48, 0\n"
"s_cselect_b32 s39, 110592, 0\n"
"s_cselect_b32 s41, 0, 110592\n"
"s_cselect_b32 s36, 516096, 0\n"
"s_cselect_b32 s38, 368640, 0\n"
"s_mov_b32 s37, 73728\n"
"s_cselect_b32 s37, 4294893568, s37\n"
"s_lshl_b32 s30, %1, 8\n"
"s_lshl_b32 s31, %1, 6\n"
"s_add_u32 s31, s31, 1024\n"
"v_add_u32 v101, s30, v96\n"
"v_add_u32 v102, s31, v96\n"
"v_lshlrev_b32 v103, 4, v101\n"
"v_mul_u32_u24 v106, 911, v101\n"
"v_lshrrev_b32 v104, 12, v106\n"
"v_and_b32 v104, 32, v104\n"
"v_xor_b32 v214, v103, v104\n"
"v_add_u32 v106, 58304, v106\n"
"v_lshrrev_b32 v104, 12, v106\n"
"v_and_b32 v104, 32, v104\n"
"v_xor_b32 v215, v103, v104\n"
"v_add_u32 v106, 58304, v106\n"
"v_lshrrev_b32 v104, 12, v106\n"
"v_and_b32 v104, 32, v104\n"
"v_xor_b32 v216, v103, v104\n"
"v_add_u32 v106, 58304, v106\n"
"v_lshrrev_b32 v104, 12, v106\n"
"v_and_b32 v104, 32, v104\n"
"v_xor_b32 v217, v103, v104\n"
"v_mul_u32_u24 v104, 911, v102\n"
"v_lshrrev_b32 v104, 12, v104\n"
"v_and_b32 v104, 32, v104\n"
"v_lshlrev_b32 v103, 4, v102\n"
"v_xor_b32 v218, v103, v104\n"
"s_cmp_eq_u32 s50, 0\n"
"s_cselect_b32 s30, 12, 0\n"
"s_cmp_eq_u32 s50, 3\n"
"s_movk_i32 s31, 1176\n"
"s_cselect_b32 s31, 1164, s31\n"
"v_bfrev_b32 v105, 1\n"
"v_cmp_gt_u32 vcc, s30, v101\n"
"s_mul_i32 s51, s48, 442368\n"
"s_mul_i32 s52, s50, 18432\n"
"v_cndmask_b32 v214, v214, v105, vcc\n"
"v_cmp_le_u32 vcc, s31, v102\n"
"s_add_u32 s51, s51, s52\n"
"s_sub_u32 s52, s51, 73920\n"
"v_cndmask_b32 v218, v218, v105, vcc\n"
"s_ashr_i32 s53, s52, 31\n"
"v_add_u32 v228, s46, v99\n"
"v_and_b32 v100, 32, v96\n"
"v_xor_b32 v229, v228, v100\n"
"v_xor_b32 v230, 32, v228\n"
"s_add_u32 s30, s48, 1\n"
"s_and_b32 s30, s30, 62\n"
"s_mov_b32 s19, 0x20000\n"
"s_mov_b32 s23, 0x20000\n"
"s_mov_b32 s27, 0x20000\n"
"s_mov_b32 s7, 0x20000\n"
"s_mov_b32 s22, 165888\n"
"s_sub_u32 s6, 28311552, s52\n"
"s_cmp_eq_u32 s30, 0\n"
"s_cselect_b32 s18, 0, s6\n"
"s_and_b32 s54, %1, 1\n"
"s_mul_i32 s59, s54, 96\n"
"s_add_u32 m0, s44, 0\n"
"s_waitcnt lgkmcnt(0)\n"
"s_add_u32 s4, s8, s52\n"
"s_addc_u32 s5, s9, s53\n"
"s_and_b32 s5, s5, 0xffff\n"
"buffer_load_dwordx4 v214, s[4:7], s36 offen offset:0 lds\n"
"s_mov_b32 s20, s10\n"
"s_and_b32 s21, s11, 0xffff\n"
"s_add_u32 s28, s12, s59\n"
"s_addc_u32 s29, s13, 0\n"
"s_add_u32 m0, s46, 61440\n"
"s_add_u32 s47, s39, 0\n"
"buffer_load_dwordx4 v228, s[20:23], s47 offen offset:0 lds\n"
"s_load_dwordx8 s[72:79], s[28:29], 0x0\n"
"s_load_dwordx8 s[80:87], s[28:29], 0x20\n"
"s_load_dwordx8 s[88:95], s[28:29], 0x40\n"
"buffer_load_dwordx4 v229, s[20:23], s47 offen offset:1024 lds\n"
"s_mov_b32 s16, s4\n"
"s_mov_b32 s17, s5\n"
"s_mov_b32 s40, 55296\n"
"buffer_load_dwordx4 v230, s[20:23], s47 offen offset:2048 lds\n"
"s_cmp_lt_u32 %1, 2\n"
"s_cselect_b32 s42, s39, 0\n"
"s_cselect_b32 s43, s41, 0\n"
"s_cselect_b32 s30, 49152, 0\n"
"s_add_u32 s42, s42, 49152\n"
"s_add_u32 s43, s43, s30\n"
"s_add_u32 m0, s44, 0\n"
"s_nop 0\n"
"buffer_load_dwordx4 v215, s[4:7], s36 offen offset:1024 lds\n"
"s_sub_u32 s26, 28311552, s51\n"
"s_add_u32 s24, s14, s51\n"
"s_addc_u32 s25, s15, 0\n"
"s_and_b32 s25, s25, 0xffff\n"
"buffer_load_dwordx4 v216, s[4:7], s36 offen offset:2048 lds\n"
"v_and_b32 v97, 15, v96\n"
"v_lshrrev_b32 v98, 4, v96\n"
"s_lshr_b32 s55, %1, 1\n"
"buffer_load_dwordx4 v217, s[4:7], s36 offen offset:3072 lds\n"
"v_lshlrev_b32 v100, 3, v98\n"
"v_add_u32 v100, s59, v100\n"
"v_lshrrev_b32 v101, 2, v97\n"
"s_add_u32 m0, s45, 16384\n"
"s_nop 0\n"
"buffer_load_dwordx4 v218, s[4:7], s36 offen lds\n"
"v_lshl_add_u32 v101, v98, 3, v101\n"
"v_mul_u32_u24 v101, 192, v101\n"
"v_add_u32 v101, 61440, v101\n"
"s_lshl_b32 s58, s58, 10\n"
"s_add_u32 s58, s58, 34816\n"
"buffer_load_dwordx4 v[236:239], v99, s[20:23], s58 offen\n"
"v_and_b32 v102, 3, v96\n"
"v_lshlrev_b32 v102, 3, v102\n"
"v_add_u32 v102, s59, v102\n"
"s_add_u32 m0, s46, 73728\n"
"s_add_u32 s47, s39, 12288\n"
"buffer_load_dwordx4 v228, s[20:23], s47 offen offset:0 lds\n"
"v_lshlrev_b32 v103, 1, v96\n"
"v_and_b32 v103, 32, v103\n"
"v_xad_u32 v231, v102, v103, v101\n"
"buffer_load_dwordx4 v229, s[20:23], s47 offen offset:1024 lds\n"
"v_add_u32 v102, 32, v102\n"
"v_xad_u32 v232, v102, v103, v101\n"
"v_add_u32 v102, 32, v102\n"
"buffer_load_dwordx4 v230, s[20:23], s47 offen offset:2048 lds\n"
"v_xad_u32 v233, v102, v103, v101\n"
"s_mul_i32 s30, s55, 9216\n"
"v_mul_u32_u24 v101, 576, v97\n"
"v_add_u32 v101, s30, v101\n"
"v_lshlrev_b32 v102, 4, v98\n"
"v_lshlrev_b32 v103, 3, v97\n"
"v_and_b32 v104, 32, v103\n"
"v_xad_u32 v212, v102, v104, v101\n"
"v_add_u32 v103, 8, v103\n"
"v_and_b32 v104, 32, v103\n"
"v_xad_u32 v213, v102, v104, v101\n"
"s_mul_i32 s30, s55, 9472\n"
"s_add_u32 s30, s30, 61440\n"
"v_mul_u32_u24 v101, 592, v97\n"
"v_add3_u32 v219, v101, s30, v100\n"
"s_mul_i32 s30, %1, 4736\n"
"s_add_u32 s30, s30, 61440\n"
"v_lshl_add_u32 v220, v96, 2, s30\n"
"v_add_u32 v101, 48, v96\n"
"v_lshrrev_b32 v101, 6, v101\n"
"v_lshl_add_u32 v221, v101, 4, v220\n"
"v_add_u32 v101, 32, v96\n"
"v_lshrrev_b32 v101, 6, v101\n"
"v_lshl_add_u32 v222, v101, 4, v220\n"
"v_add_u32 v101, 16, v96\n"
"v_lshrrev_b32 v101, 6, v101\n"
"v_lshl_add_u32 v223, v101, 4, v220\n"
"s_mul_i32 s31, %1, 4608\n"
"v_lshl_add_u32 v225, v96, 2, s31\n"
"v_add_u32 v226, 4096, v225\n"
"s_waitcnt lgkmcnt(0)\n"
"s_bfm_b64 exec, 16, 0\n"
"v_mov_b32 v108, s72\n"
"v_mov_b32 v109, s73\n"
"v_mov_b32 v110, s80\n"
"v_mov_b32 v111, s81\n"
"v_mov_b32 v112, s88\n"
"v_mov_b32 v113, s89\n"
"s_bfm_b64 exec, 16, 16\n"
"v_mov_b32 v108, s74\n"
"v_mov_b32 v109, s75\n"
"v_mov_b32 v110, s82\n"
"v_mov_b32 v111, s83\n"
"v_mov_b32 v112, s90\n"
"v_mov_b32 v113, s91\n"
"s_bfm_b64 exec, 16, 32\n"
"v_mov_b32 v108, s76\n"
"v_mov_b32 v109, s77\n"
"v_mov_b32 v110, s84\n"
"v_mov_b32 v111, s85\n"
"v_mov_b32 v112, s92\n"
"v_mov_b32 v113, s93\n"
"s_bfm_b64 exec, 16, 48\n"
"v_mov_b32 v108, s78\n"
"v_mov_b32 v109, s79\n"
"v_mov_b32 v110, s86\n"
"v_mov_b32 v111, s87\n"
"v_mov_b32 v112, s94\n"
"v_mov_b32 v113, s95\n"
"s_mov_b64 exec, -1\n"
"v_lshlrev_b32 v132, 16, v108\n"
"v_and_b32 v133, 0xffff0000, v108\n"
"v_lshlrev_b32 v134, 16, v109\n"
"v_and_b32 v135, 0xffff0000, v109\n"
"v_lshlrev_b32 v136, 16, v110\n"
"v_and_b32 v137, 0xffff0000, v110\n"
"v_lshlrev_b32 v138, 16, v111\n"
"v_and_b32 v139, 0xffff0000, v111\n"
"v_lshlrev_b32 v140, 16, v112\n"
"v_and_b32 v141, 0xffff0000, v112\n"
"v_lshlrev_b32 v142, 16, v113\n"
"v_and_b32 v143, 0xffff0000, v113\n"
"s_mov_b32 s28, s30\n"
"s_mov_b32 s29, s31\n"
"s_mov_b64 vcc, -1\n"
"s_cmp_eq_u32 %1, 0\n"
"s_cbranch_scc1 .Lconv_stub0\n"
"s_cmp_eq_u32 %1, 1\n"
"s_cbranch_scc1 .Lconv_stub1\n"
"s_cmp_eq_u32 %1, 2\n"
"s_cbranch_scc1 .Lconv_stub2\n"
"s_branch .Lconv_stub3\n"
".Lconv_ret:\n"
"s_mov_b64 vcc, 0\n"
"s_add_u32 m0, s46, 86016\n"
"s_add_u32 s47, s39, 24576\n"
"buffer_load_dwordx4 v228, s[20:23], s47 offen offset:0 lds\n"
"buffer_load_dwordx4 v229, s[20:23], s47 offen offset:1024 lds\n"
"buffer_load_dwordx4 v230, s[20:23], s47 offen offset:2048 lds\n"
"s_waitcnt vmcnt(11)\n"
"s_barrier\n"
"ds_read_b64_tr_b16 a[0:1], v231 offset:0\n"
"ds_read_b64_tr_b16 a[2:3], v231 offset:768\n"
"ds_read_b64_tr_b16 a[4:5], v232 offset:0\n"
"ds_read_b64_tr_b16 a[6:7], v232 offset:768\n"
"ds_read_b64_tr_b16 a[8:9], v233 offset:0\n"
"ds_read_b64_tr_b16 a[10:11], v233 offset:768\n"
"ds_read_b64_tr_b16 a[12:13], v231 offset:6144\n"
"ds_read_b64_tr_b16 a[14:15], v231 offset:6912\n"
"ds_read_b64_tr_b16 a[16:17], v232 offset:6144\n"
"ds_read_b64_tr_b16 a[18:19], v232 offset:6912\n"
"ds_read_b64_tr_b16 a[20:21], v233 offset:6144\n"
"ds_read_b64_tr_b16 a[22:23], v233 offset:6912\n"
"s_add_u32 m0, s46, 98304\n"
"s_add_u32 s47, s39, 36864\n"
"buffer_load_dwordx4 v228, s[20:23], s47 offen offset:0 lds\n"
"buffer_load_dwordx4 v229, s[20:23], s47 offen offset:1024 lds\n"
"buffer_load_dwordx4 v230, s[20:23], s47 offen offset:2048 lds\n"
"s_waitcnt vmcnt(10)\n"
"s_barrier\n"
"ds_read_b128 v[36:39], v212 offset:0\n"
"ds_read_b128 v[48:51], v212 offset:192\n"
"ds_read_b128 v[60:63], v212 offset:384\n"
"ds_read_b128 v[40:43], v212 offset:64\n"
"ds_read_b128 v[52:55], v212 offset:256\n"
"ds_read_b128 v[64:67], v212 offset:448\n"
"s_waitcnt lgkmcnt(3)\n"
"v_mfma_f32_16x16x32_bf16 v[0:3], a[0:3], v[36:39], v[132:135]\n"
"ds_read_b128 v[44:47], v212 offset:128\n"
"s_add_u32 m0, s46, 110592\n"
"v_mfma_f32_16x16x32_bf16 v[4:7], a[4:7], v[36:39], v[136:139]\n"
"buffer_load_dwordx4 v228, s[20:23], s42 offen offset:0 lds\n"
"v_mfma_f32_16x16x32_bf16 v[8:11], a[8:11], v[36:39], v[140:143]\n"
"ds_read_b128 v[56:59], v212 offset:320\n"
"v_mfma_f32_16x16x32_bf16 v[20:23], a[8:11], v[48:51], v[140:143]\n"
"ds_read_b128 v[68:71], v212 offset:512\n"
"v_mfma_f32_16x16x32_bf16 v[16:19], a[4:7], v[48:51], v[136:139]\n"
"ds_read_b128 v[72:75], v213 offset:576\n"
"v_mfma_f32_16x16x32_bf16 v[12:15], a[0:3], v[48:51], v[132:135]\n"
"buffer_load_dwordx4 v229, s[20:23], s42 offen offset:1024 lds\n"
"v_mfma_f32_16x16x32_bf16 v[24:27], a[0:3], v[60:63], v[132:135]\n"
"s_waitcnt vmcnt(8) lgkmcnt(3)\n"
"s_barrier\n"
"v_mfma_f32_16x16x32_bf16 v[28:31], a[4:7], v[60:63], v[136:139]\n"
"ds_read_b64_tr_b16 a[24:25], v231 offset:12288\n"
"ds_read_b64_tr_b16 a[26:27], v231 offset:13056\n"
"v_mfma_f32_16x16x32_bf16 v[32:35], a[8:11], v[60:63], v[140:143]\n"
"ds_read_b64_tr_b16 a[28:29], v232 offset:12288\n"
"v_mfma_f32_16x16x32_bf16 v[0:3], a[12:15], v[40:43], v[0:3]\n"
"buffer_load_dwordx4 v230, s[20:23], s42 offen offset:2048 lds\n"
"v_mfma_f32_16x16x32_bf16 v[4:7], a[16:19], v[40:43], v[4:7]\n"
"ds_read_b64_tr_b16 a[30:31], v232 offset:13056\n"
"ds_read_b64_tr_b16 a[32:33], v233 offset:12288\n"
"v_mfma_f32_16x16x32_bf16 v[8:11], a[20:23], v[40:43], v[8:11]\n"
"ds_read_b64_tr_b16 a[34:35], v233 offset:13056\n"
"s_add_u32 m0, s44, 20480\n"
"v_mfma_f32_16x16x32_bf16 v[20:23], a[20:23], v[52:55], v[20:23]\n"
"ds_read_b64_tr_b16 a[36:37], v231 offset:18432\n"
"s_add_u32 s36, s36, s37\n"
"v_mfma_f32_16x16x32_bf16 v[16:19], a[16:19], v[52:55], v[16:19]\n"
"buffer_load_dwordx4 v214, s[4:7], s36 offen offset:0 lds\n"
"v_mfma_f32_16x16x32_bf16 v[12:15], a[12:15], v[52:55], v[12:15]\n"
"ds_read_b64_tr_b16 a[38:39], v231 offset:19200\n"
"v_mfma_f32_16x16x32_bf16 v[24:27], a[12:15], v[64:67], v[24:27]\n"
"ds_read_b64_tr_b16 a[40:41], v232 offset:18432\n"
"v_mfma_f32_16x16x32_bf16 v[28:31], a[16:19], v[64:67], v[28:31]\n"
"ds_read_b64_tr_b16 a[42:43], v232 offset:19200\n"
"v_mfma_f32_16x16x32_bf16 v[32:35], a[20:23], v[64:67], v[32:35]\n"
"ds_read_b64_tr_b16 a[44:45], v233 offset:18432\n"
"s_waitcnt lgkmcnt(5)\n"
"v_mfma_f32_16x16x32_bf16 v[0:3], a[24:27], v[44:47], v[0:3]\n"
"buffer_load_dwordx4 v215, s[4:7], s36 offen offset:1024 lds\n"
"v_mfma_f32_16x16x32_bf16 v[4:7], a[28:31], v[44:47], v[4:7]\n"
"ds_read_b64_tr_b16 a[46:47], v233 offset:19200\n"
"v_mfma_f32_16x16x32_bf16 v[8:11], a[32:35], v[44:47], v[8:11]\n"
"ds_read_b128 v[76:79], v213 offset:640\n"
"v_mfma_f32_16x16x32_bf16 v[20:23], a[32:35], v[56:59], v[20:23]\n"
"ds_read_b128 v[80:83], v213 offset:704\n"
"v_mfma_f32_16x16x32_bf16 v[16:19], a[28:31], v[56:59], v[16:19]\n"
"buffer_load_dwordx4 v216, s[4:7], s36 offen offset:2048 lds\n"
"v_mfma_f32_16x16x32_bf16 v[12:15], a[24:27], v[56:59], v[12:15]\n"
"ds_read_b128 v[84:87], v213 offset:768\n"
"v_mfma_f32_16x16x32_bf16 v[24:27], a[24:27], v[68:71], v[24:27]\n"
"ds_read_b128 v[88:91], v213 offset:832\n"
"v_mfma_f32_16x16x32_bf16 v[28:31], a[28:31], v[68:71], v[28:31]\n"
"s_waitcnt vmcnt(6) lgkmcnt(4)\n"
"s_barrier\n"
"v_mfma_f32_16x16x32_bf16 v[32:35], a[32:35], v[68:71], v[32:35]\n"
"ds_read_b64_tr_b16 a[48:49], v231 offset:24576\n"
"v_mfma_f32_16x16x32_bf16 v[0:3], a[36:39], v[48:51], v[0:3]\n"
"buffer_load_dwordx4 v217, s[4:7], s36 offen offset:3072 lds\n"
"v_mfma_f32_16x16x32_bf16 v[4:7], a[40:43], v[48:51], v[4:7]\n"
"ds_read_b64_tr_b16 a[50:51], v231 offset:25344\n"
"ds_read_b64_tr_b16 a[52:53], v232 offset:24576\n"
"v_mfma_f32_16x16x32_bf16 v[8:11], a[44:47], v[48:51], v[8:11]\n"
"ds_read_b64_tr_b16 a[54:55], v232 offset:25344\n"
"ds_read_b64_tr_b16 a[56:57], v233 offset:24576\n"
"v_mfma_f32_16x16x32_bf16 v[20:23], a[44:47], v[60:63], v[20:23]\n"
"ds_read_b64_tr_b16 a[58:59], v233 offset:25344\n"
"ds_read_b64_tr_b16 a[60:61], v231 offset:30720\n"
"v_mfma_f32_16x16x32_bf16 v[16:19], a[40:43], v[60:63], v[16:19]\n"
"ds_read_b64_tr_b16 a[62:63], v231 offset:31488\n"
"s_add_u32 m0, s45, 36864\n"
"v_mfma_f32_16x16x32_bf16 v[12:15], a[36:39], v[60:63], v[12:15]\n"
"buffer_load_dwordx4 v218, s[4:7], s36 offen lds\n"
"v_mfma_f32_16x16x32_bf16 v[24:27], a[36:39], v[72:75], v[24:27]\n"
"ds_read_b64_tr_b16 a[64:65], v232 offset:30720\n"
"s_add_u32 m0, s46, 122880\n"
"v_mfma_f32_16x16x32_bf16 v[28:31], a[40:43], v[72:75], v[28:31]\n"
"ds_read_b64_tr_b16 a[66:67], v232 offset:31488\n"
"s_add_u32 s47, s40, 6144\n"
"v_mfma_f32_16x16x32_bf16 v[32:35], a[44:47], v[72:75], v[32:35]\n"
"ds_read_b64_tr_b16 a[68:69], v233 offset:30720\n"
"s_waitcnt lgkmcnt(5)\n"
"v_mfma_f32_16x16x32_bf16 v[0:3], a[48:51], v[52:55], v[0:3]\n"
"buffer_load_dwordx4 v228, s[20:23], s47 offen offset:0 lds\n"
"v_mfma_f32_16x16x32_bf16 v[4:7], a[52:55], v[52:55], v[4:7]\n"
"ds_read_b64_tr_b16 a[70:71], v233 offset:31488\n"
"v_mfma_f32_16x16x32_bf16 v[8:11], a[56:59], v[52:55], v[8:11]\n"
"ds_read_b64_tr_b16 a[72:73], v231 offset:36864\n"
"v_mfma_f32_16x16x32_bf16 v[20:23], a[56:59], v[64:67], v[20:23]\n"
"ds_read_b64_tr_b16 a[74:75], v231 offset:37632\n"
"v_mfma_f32_16x16x32_bf16 v[16:19], a[52:55], v[64:67], v[16:19]\n"
"buffer_load_dwordx4 v229, s[20:23], s47 offen offset:1024 lds\n"
"v_mfma_f32_16x16x32_bf16 v[12:15], a[48:51], v[64:67], v[12:15]\n"
"ds_read_b64_tr_b16 a[76:77], v232 offset:36864\n"
"v_mfma_f32_16x16x32_bf16 v[24:27], a[48:51], v[76:79], v[24:27]\n"
"ds_read_b64_tr_b16 a[78:79], v232 offset:37632\n"
"v_mfma_f32_16x16x32_bf16 v[28:31], a[52:55], v[76:79], v[28:31]\n"
"ds_read_b64_tr_b16 a[80:81], v233 offset:36864\n"
"v_mfma_f32_16x16x32_bf16 v[32:35], a[56:59], v[76:79], v[32:35]\n"
"ds_read_b64_tr_b16 a[82:83], v233 offset:37632\n"
"s_waitcnt lgkmcnt(6)\n"
"v_mfma_f32_16x16x32_bf16 v[0:3], a[60:63], v[56:59], v[0:3]\n"
"buffer_load_dwordx4 v230, s[20:23], s47 offen offset:2048 lds\n"
"v_mfma_f32_16x16x32_bf16 v[4:7], a[64:67], v[56:59], v[4:7]\n"
"ds_read_b64_tr_b16 a[84:85], v231 offset:43008\n"
"s_add_u32 m0, s46, 135168\n"
"v_mfma_f32_16x16x32_bf16 v[8:11], a[68:71], v[56:59], v[8:11]\n"
"ds_read_b64_tr_b16 a[86:87], v231 offset:43776\n"
"s_add_u32 s47, s40, 18432\n"
"v_mfma_f32_16x16x32_bf16 v[20:23], a[68:71], v[68:71], v[20:23]\n"
"ds_read_b64_tr_b16 a[88:89], v232 offset:43008\n"
"v_mfma_f32_16x16x32_bf16 v[16:19], a[64:67], v[68:71], v[16:19]\n"
"buffer_load_dwordx4 v228, s[20:23], s47 offen offset:0 lds\n"
"v_mfma_f32_16x16x32_bf16 v[12:15], a[60:63], v[68:71], v[12:15]\n"
"ds_read_b64_tr_b16 a[90:91], v232 offset:43776\n"
"v_mfma_f32_16x16x32_bf16 v[24:27], a[60:63], v[80:83], v[24:27]\n"
"ds_read_b64_tr_b16 a[92:93], v233 offset:43008\n"
"v_mfma_f32_16x16x32_bf16 v[28:31], a[64:67], v[80:83], v[28:31]\n"
"ds_read_b64_tr_b16 a[94:95], v233 offset:43776\n"
"v_mfma_f32_16x16x32_bf16 v[32:35], a[68:71], v[80:83], v[32:35]\n"
"ds_read_b128 v[92:95], v213 offset:896\n"
"s_waitcnt lgkmcnt(7)\n"
"v_mfma_f32_16x16x32_bf16 v[0:3], a[72:75], v[60:63], v[0:3]\n"
"buffer_load_dwordx4 v229, s[20:23], s47 offen offset:1024 lds\n"
"v_mfma_f32_16x16x32_bf16 v[4:7], a[76:79], v[60:63], v[4:7]\n"
"v_mfma_f32_16x16x32_bf16 v[8:11], a[80:83], v[60:63], v[8:11]\n"
"v_mfma_f32_16x16x32_bf16 v[20:23], a[80:83], v[72:75], v[20:23]\n"
"v_mfma_f32_16x16x32_bf16 v[16:19], a[76:79], v[72:75], v[16:19]\n"
"buffer_load_dwordx4 v230, s[20:23], s47 offen offset:2048 lds\n"
"v_mfma_f32_16x16x32_bf16 v[12:15], a[72:75], v[72:75], v[12:15]\n"
"s_add_u32 m0, s46, 147456\n"
"s_add_u32 s47, s40, 30720\n"
"v_mfma_f32_16x16x32_bf16 v[24:27], a[72:75], v[84:87], v[24:27]\n"
"s_waitcnt vmcnt(6) lgkmcnt(0)\n"
"s_barrier\n"
"v_mfma_f32_16x16x32_bf16 v[28:31], a[76:79], v[84:87], v[28:31]\n"
"ds_read_b64_tr_b16 a[96:97], v231 offset:49152\n"
"ds_read_b64_tr_b16 a[98:99], v231 offset:49920\n"
"v_mfma_f32_16x16x32_bf16 v[32:35], a[80:83], v[84:87], v[32:35]\n"
"ds_read_b64_tr_b16 a[100:101], v232 offset:49152\n"
"v_mfma_f32_16x16x32_bf16 v[0:3], a[84:87], v[64:67], v[0:3]\n"
"buffer_load_dwordx4 v228, s[20:23], s47 offen offset:0 lds\n"
"v_mfma_f32_16x16x32_bf16 v[4:7], a[88:91], v[64:67], v[4:7]\n"
"ds_read_b64_tr_b16 a[102:103], v232 offset:49920\n"
"ds_read_b64_tr_b16 a[104:105], v233 offset:49152\n"
"v_mfma_f32_16x16x32_bf16 v[8:11], a[92:95], v[64:67], v[8:11]\n"
"ds_read_b64_tr_b16 a[106:107], v233 offset:49920\n"
"v_mfma_f32_16x16x32_bf16 v[20:23], a[92:95], v[76:79], v[20:23]\n"
"ds_read_b128 v[108:111], v212 offset:20480\n"
"v_mfma_f32_16x16x32_bf16 v[16:19], a[88:91], v[76:79], v[16:19]\n"
"buffer_load_dwordx4 v229, s[20:23], s47 offen offset:1024 lds\n"
"v_mfma_f32_16x16x32_bf16 v[12:15], a[84:87], v[76:79], v[12:15]\n"
"ds_read_b128 v[112:115], v212 offset:20544\n"
"v_mfma_f32_16x16x32_bf16 v[24:27], a[84:87], v[88:91], v[24:27]\n"
"ds_read_b128 v[116:119], v212 offset:20608\n"
"v_mfma_f32_16x16x32_bf16 v[28:31], a[88:91], v[88:91], v[28:31]\n"
"ds_read_b128 v[120:123], v212 offset:20672\n"
"v_mfma_f32_16x16x32_bf16 v[32:35], a[92:95], v[88:91], v[32:35]\n"
"ds_read_b128 v[124:127], v212 offset:20736\n"
"s_waitcnt lgkmcnt(5)\n"
"v_mfma_f32_16x16x32_bf16 v[0:3], a[96:99], v[68:71], v[0:3]\n"
"buffer_load_dwordx4 v230, s[20:23], s47 offen offset:2048 lds\n"
"v_mfma_f32_16x16x32_bf16 v[4:7], a[100:103], v[68:71], v[4:7]\n"
"ds_read_b128 v[128:131], v212 offset:20800\n"
"s_add_u32 m0, s46, 61440\n"
"v_mfma_f32_16x16x32_bf16 v[8:11], a[104:107], v[68:71], v[8:11]\n"
"ds_read_b64_tr_b16 a[108:109], v231 offset:55296\n"
"s_add_u32 s47, s40, 43008\n"
"v_mfma_f32_16x16x32_bf16 v[20:23], a[104:107], v[80:83], v[20:23]\n"
"ds_read_b64_tr_b16 a[110:111], v231 offset:56064\n"
"v_mfma_f32_16x16x32_bf16 v[16:19], a[100:103], v[80:83], v[16:19]\n"
"buffer_load_dwordx4 v228, s[20:23], s47 offen offset:0 lds\n"
"v_mfma_f32_16x16x32_bf16 v[12:15], a[96:99], v[80:83], v[12:15]\n"
"ds_read_b64_tr_b16 a[112:113], v232 offset:55296\n"
"v_mfma_f32_16x16x32_bf16 v[24:27], a[96:99], v[92:95], v[24:27]\n"
"ds_read_b64_tr_b16 a[114:115], v232 offset:56064\n"
"v_mfma_f32_16x16x32_bf16 v[28:31], a[100:103], v[92:95], v[28:31]\n"
"ds_read_b64_tr_b16 a[116:117], v233 offset:55296\n"
"v_mfma_f32_16x16x32_bf16 v[32:35], a[104:107], v[92:95], v[32:35]\n"
"ds_read_b64_tr_b16 a[118:119], v233 offset:56064\n"
"s_waitcnt lgkmcnt(9)\n"
"v_mfma_f32_16x16x32_bf16 v[36:39], a[0:3], v[108:111], v[132:135]\n"
"buffer_load_dwordx4 v229, s[20:23], s47 offen offset:1024 lds\n"
"v_mfma_f32_16x16x32_bf16 v[36:39], a[12:15], v[112:115], v[36:39]\n"
"v_mfma_f32_16x16x32_bf16 v[36:39], a[24:27], v[116:119], v[36:39]\n"
"v_mfma_f32_16x16x32_bf16 v[40:43], a[28:31], v[116:119], v[136:139]\n"
"v_mfma_f32_16x16x32_bf16 v[40:43], a[16:19], v[112:115], v[40:43]\n"
"buffer_load_dwordx4 v230, s[20:23], s47 offen offset:2048 lds\n"
"v_mfma_f32_16x16x32_bf16 v[40:43], a[4:7], v[108:111], v[40:43]\n"
"s_add_u32 m0, s44, 40960\n"
"s_add_u32 s36, s36, s37\n"
"v_mfma_f32_16x16x32_bf16 v[44:47], a[8:11], v[108:111], v[140:143]\n"
"v_mfma_f32_16x16x32_bf16 v[44:47], a[20:23], v[112:115], v[44:47]\n"
"v_mfma_f32_16x16x32_bf16 v[44:47], a[32:35], v[116:119], v[44:47]\n"
"ds_read_b128 v[108:111], v212 offset:20864\n"
"s_waitcnt lgkmcnt(7)\n"
"v_mfma_f32_16x16x32_bf16 v[36:39], a[36:39], v[120:123], v[36:39]\n"
"buffer_load_dwordx4 v214, s[4:7], s36 offen offset:0 lds\n"
"v_mfma_f32_16x16x32_bf16 v[36:39], a[48:51], v[124:127], v[36:39]\n"
"ds_read_b128 v[112:115], v212 offset:20928\n"
"v_mfma_f32_16x16x32_bf16 v[36:39], a[60:63], v[128:131], v[36:39]\n"
"ds_read_b128 v[116:119], v212 offset:20992\n"
"v_mfma_f32_16x16x32_bf16 v[40:43], a[64:67], v[128:131], v[40:43]\n"
"v_mfma_f32_16x16x32_bf16 v[40:43], a[52:55], v[124:127], v[40:43]\n"
"buffer_load_dwordx4 v215, s[4:7], s36 offen offset:1024 lds\n"
"v_mfma_f32_16x16x32_bf16 v[40:43], a[40:43], v[120:123], v[40:43]\n"
"v_mfma_f32_16x16x32_bf16 v[44:47], a[44:47], v[120:123], v[44:47]\n"
"v_mfma_f32_16x16x32_bf16 v[44:47], a[56:59], v[124:127], v[44:47]\n"
"v_mfma_f32_16x16x32_bf16 v[44:47], a[68:71], v[128:131], v[44:47]\n"
"buffer_load_dwordx4 v216, s[4:7], s36 offen offset:2048 lds\n"
"v_mfma_f32_16x16x32_bf16 v[48:51], a[24:27], v[128:131], v[132:135]\n"
"s_waitcnt vmcnt(9) lgkmcnt(0)\n"
"s_barrier\n"
"v_mfma_f32_16x16x32_bf16 v[48:51], a[12:15], v[124:127], v[48:51]\n"
"ds_read_b64_tr_b16 a[120:121], v231 offset:61440\n"
"v_mfma_f32_16x16x32_bf16 v[48:51], a[0:3], v[120:123], v[48:51]\n"
"ds_read_b64_tr_b16 a[122:123], v231 offset:62208\n"
"v_mfma_f32_16x16x32_bf16 v[52:55], a[4:7], v[120:123], v[136:139]\n"
"buffer_load_dwordx4 v217, s[4:7], s36 offen offset:3072 lds\n"
"v_mfma_f32_16x16x32_bf16 v[52:55], a[16:19], v[124:127], v[52:55]\n"
"ds_read_b64_tr_b16 a[124:125], v232 offset:61440\n"
"s_add_u32 m0, s45, 57344\n"
"v_mfma_f32_16x16x32_bf16 v[52:55], a[28:31], v[128:131], v[52:55]\n"
"ds_read_b64_tr_b16 a[126:127], v232 offset:62208\n"
"v_mfma_f32_16x16x32_bf16 v[56:59], a[32:35], v[128:131], v[140:143]\n"
"ds_read_b64_tr_b16 a[128:129], v233 offset:61440\n"
"v_mfma_f32_16x16x32_bf16 v[56:59], a[20:23], v[124:127], v[56:59]\n"
"buffer_load_dwordx4 v218, s[4:7], s36 offen lds\n"
"v_mfma_f32_16x16x32_bf16 v[56:59], a[8:11], v[120:123], v[56:59]\n"
"ds_read_b128 v[120:123], v213 offset:21056\n"
"v_mfma_f32_16x16x32_bf16 v[36:39], a[72:75], v[108:111], v[36:39]\n"
"ds_read_b128 v[124:127], v213 offset:21120\n"
"s_add_u32 m0, s46, 73728\n"
"v_mfma_f32_16x16x32_bf16 v[36:39], a[84:87], v[112:115], v[36:39]\n"
"ds_read_b128 v[128:131], v213 offset:21184\n"
"s_add_u32 s47, s41, 0\n"
"v_mfma_f32_16x16x32_bf16 v[36:39], a[96:99], v[116:119], v[36:39]\n"
"buffer_load_dwordx4 v228, s[20:23], s47 offen offset:0 lds\n"
"v_mfma_f32_16x16x32_bf16 v[40:43], a[100:103], v[116:119], v[40:43]\n"
"ds_read_b64_tr_b16 a[130:131], v233 offset:62208\n"
"v_add_u32 v231, 49152, v231\n"
"v_mfma_f32_16x16x32_bf16 v[40:43], a[88:91], v[112:115], v[40:43]\n"
"v_add_u32 v232, 49152, v232\n"
"v_mfma_f32_16x16x32_bf16 v[40:43], a[76:79], v[108:111], v[40:43]\n"
"v_add_u32 v233, 49152, v233\n"
"v_mfma_f32_16x16x32_bf16 v[44:47], a[80:83], v[108:111], v[44:47]\n"
"buffer_load_dwordx4 v229, s[20:23], s47 offen offset:1024 lds\n"
"v_mfma_f32_16x16x32_bf16 v[44:47], a[92:95], v[112:115], v[44:47]\n"
"ds_read_b64_tr_b16 a[132:133], v231 offset:18432\n"
"v_mfma_f32_16x16x32_bf16 v[44:47], a[104:107], v[116:119], v[44:47]\n"
"ds_read_b64_tr_b16 a[134:135], v231 offset:19200\n"
"v_mfma_f32_16x16x32_bf16 v[48:51], a[60:63], v[116:119], v[48:51]\n"
"ds_read_b64_tr_b16 a[136:137], v232 offset:18432\n"
"v_mfma_f32_16x16x32_bf16 v[48:51], a[48:51], v[112:115], v[48:51]\n"
"buffer_load_dwordx4 v230, s[20:23], s47 offen offset:2048 lds\n"
"v_mfma_f32_16x16x32_bf16 v[48:51], a[36:39], v[108:111], v[48:51]\n"
"ds_read_b64_tr_b16 a[138:139], v232 offset:19200\n"
"s_add_u32 m0, s46, 86016\n"
"v_mfma_f32_16x16x32_bf16 v[52:55], a[40:43], v[108:111], v[52:55]\n"
"ds_read_b64_tr_b16 a[140:141], v233 offset:18432\n"
"s_add_u32 s47, s41, 12288\n"
"v_mfma_f32_16x16x32_bf16 v[52:55], a[52:55], v[112:115], v[52:55]\n"
"ds_read_b64_tr_b16 a[142:143], v233 offset:19200\n"
"v_mfma_f32_16x16x32_bf16 v[52:55], a[64:67], v[116:119], v[52:55]\n"
"buffer_load_dwordx4 v228, s[20:23], s47 offen offset:0 lds\n"
"v_mfma_f32_16x16x32_bf16 v[56:59], a[68:71], v[116:119], v[56:59]\n"
"ds_read_b64_tr_b16 a[144:145], v231 offset:24576\n"
"v_mfma_f32_16x16x32_bf16 v[56:59], a[56:59], v[112:115], v[56:59]\n"
"ds_read_b64_tr_b16 a[146:147], v231 offset:25344\n"
"v_mfma_f32_16x16x32_bf16 v[56:59], a[44:47], v[108:111], v[56:59]\n"
"ds_read_b64_tr_b16 a[148:149], v232 offset:24576\n"
"v_mfma_f32_16x16x32_bf16 v[60:63], a[0:3], v[108:111], v[132:135]\n"
"buffer_load_dwordx4 v229, s[20:23], s47 offen offset:1024 lds\n"
"v_mfma_f32_16x16x32_bf16 v[60:63], a[12:15], v[112:115], v[60:63]\n"
"ds_read_b64_tr_b16 a[150:151], v232 offset:25344\n"
"v_mfma_f32_16x16x32_bf16 v[60:63], a[24:27], v[116:119], v[60:63]\n"
"s_waitcnt vmcnt(10) lgkmcnt(4)\n"
"s_barrier\n"
"v_mfma_f32_16x16x32_bf16 v[64:67], a[28:31], v[116:119], v[136:139]\n"
"ds_read_b64_tr_b16 a[152:153], v233 offset:24576\n"
"v_mfma_f32_16x16x32_bf16 v[64:67], a[16:19], v[112:115], v[64:67]\n"
"buffer_load_dwordx4 v230, s[20:23], s47 offen offset:2048 lds\n"
"v_mfma_f32_16x16x32_bf16 v[64:67], a[4:7], v[108:111], v[64:67]\n"
"ds_read_b64_tr_b16 a[154:155], v233 offset:25344\n"
"v_mfma_f32_16x16x32_bf16 v[68:71], a[8:11], v[108:111], v[140:143]\n"
"ds_read_b64_tr_b16 a[156:157], v231 offset:30720\n"
"v_mfma_f32_16x16x32_bf16 v[68:71], a[20:23], v[112:115], v[68:71]\n"
"ds_read_b64_tr_b16 a[158:159], v231 offset:31488\n"
"v_mfma_f32_16x16x32_bf16 v[68:71], a[32:35], v[116:119], v[68:71]\n"
"ds_read_b128 v[108:111], v213 offset:21248\n"
"v_mfma_f32_16x16x32_bf16 v[48:51], a[72:75], v[120:123], v[48:51]\n"
"ds_read_b128 v[112:115], v213 offset:21312\n"
"v_mfma_f32_16x16x32_bf16 v[48:51], a[84:87], v[124:127], v[48:51]\n"
"ds_read_b128 v[116:119], v213 offset:21376\n"
"v_mfma_f32_16x16x32_bf16 v[48:51], a[96:99], v[128:131], v[48:51]\n"
"ds_read_b64_tr_b16 a[160:161], v232 offset:30720\n"
"v_mfma_f32_16x16x32_bf16 v[52:55], a[100:103], v[128:131], v[52:55]\n"
"ds_read_b64_tr_b16 a[162:163], v232 offset:31488\n"
"v_mfma_f32_16x16x32_bf16 v[52:55], a[88:91], v[124:127], v[52:55]\n"
"ds_read_b64_tr_b16 a[164:165], v233 offset:30720\n"
"v_mfma_f32_16x16x32_bf16 v[52:55], a[76:79], v[120:123], v[52:55]\n"
"ds_read_b64_tr_b16 a[166:167], v233 offset:31488\n"
"v_mfma_f32_16x16x32_bf16 v[56:59], a[80:83], v[120:123], v[56:59]\n"
"ds_read_b64_tr_b16 a[168:169], v231 offset:36864\n"
"v_mfma_f32_16x16x32_bf16 v[56:59], a[92:95], v[124:127], v[56:59]\n"
"ds_read_b64_tr_b16 a[170:171], v231 offset:37632\n"
"v_mfma_f32_16x16x32_bf16 v[56:59], a[104:107], v[128:131], v[56:59]\n"
"ds_read_b64_tr_b16 a[172:173], v232 offset:36864\n"
"v_mfma_f32_16x16x32_bf16 v[60:63], a[60:63], v[128:131], v[60:63]\n"
"ds_read_b64_tr_b16 a[174:175], v232 offset:37632\n"
"v_mfma_f32_16x16x32_bf16 v[60:63], a[48:51], v[124:127], v[60:63]\n"
"ds_read_b64_tr_b16 a[176:177], v233 offset:36864\n"
"v_mfma_f32_16x16x32_bf16 v[60:63], a[36:39], v[120:123], v[60:63]\n"
"ds_read_b64_tr_b16 a[178:179], v233 offset:37632\n"
"v_mfma_f32_16x16x32_bf16 v[64:67], a[40:43], v[120:123], v[64:67]\n"
"ds_read_b64_tr_b16 a[180:181], v231 offset:43008\n"
"v_mfma_f32_16x16x32_bf16 v[64:67], a[52:55], v[124:127], v[64:67]\n"
"ds_read_b64_tr_b16 a[182:183], v231 offset:43776\n"
"v_mfma_f32_16x16x32_bf16 v[64:67], a[64:67], v[128:131], v[64:67]\n"
"ds_read_b64_tr_b16 a[184:185], v232 offset:43008\n"
"v_mfma_f32_16x16x32_bf16 v[68:71], a[68:71], v[128:131], v[68:71]\n"
"ds_read_b64_tr_b16 a[186:187], v232 offset:43776\n"
"v_mfma_f32_16x16x32_bf16 v[68:71], a[56:59], v[124:127], v[68:71]\n"
"ds_read_b64_tr_b16 a[188:189], v233 offset:43008\n"
"v_mfma_f32_16x16x32_bf16 v[68:71], a[44:47], v[120:123], v[68:71]\n"
"ds_read_b128 v[120:123], v212 offset:20480\n"
"s_waitcnt lgkmcnt(15)\n"
".Lconv_stub0:\n"
"s_cbranch_vccnz .Lconv_ret\n"
"v_mfma_f32_16x16x32_bf16 v[60:63], a[72:75], v[108:111], v[60:63]\n"
"ds_read_b128 v[124:127], v212 offset:20544\n"
"v_mfma_f32_16x16x32_bf16 v[60:63], a[84:87], v[112:115], v[60:63]\n"
"ds_read_b128 v[128:131], v212 offset:20608\n"
"v_mfma_f32_16x16x32_bf16 v[60:63], a[96:99], v[116:119], v[60:63]\n"
"ds_read_b64_tr_b16 a[190:191], v233 offset:43776\n"
"v_add_u32 v231, 4294918144, v231\n"
"v_mfma_f32_16x16x32_bf16 v[64:67], a[100:103], v[116:119], v[64:67]\n"
"v_add_u32 v232, 4294918144, v232\n"
"v_mfma_f32_16x16x32_bf16 v[64:67], a[88:91], v[112:115], v[64:67]\n"
"v_add_u32 v233, 4294918144, v233\n"
"v_mfma_f32_16x16x32_bf16 v[64:67], a[76:79], v[108:111], v[64:67]\n"
"ds_read_b64_tr_b16 a[192:193], v231 offset:0\n"
"v_mfma_f32_16x16x32_bf16 v[68:71], a[80:83], v[108:111], v[68:71]\n"
"ds_read_b64_tr_b16 a[194:195], v231 offset:768\n"
"v_mfma_f32_16x16x32_bf16 v[68:71], a[92:95], v[112:115], v[68:71]\n"
"ds_read_b64_tr_b16 a[196:197], v232 offset:0\n"
"v_mfma_f32_16x16x32_bf16 v[68:71], a[104:107], v[116:119], v[68:71]\n"
"ds_read_b128 v[108:111], v212 offset:20672\n"
"s_waitcnt lgkmcnt(5)\n"
"v_mfma_f32_16x16x32_bf16 v[0:3], a[108:111], v[120:123], v[0:3]\n"
"ds_read_b128 v[112:115], v212 offset:20736\n"
"v_mfma_f32_16x16x32_bf16 v[0:3], a[120:123], v[124:127], v[0:3]\n"
"ds_read_b128 v[116:119], v212 offset:20800\n"
"v_mfma_f32_16x16x32_bf16 v[0:3], a[132:135], v[128:131], v[0:3]\n"
"ds_read_b64_tr_b16 a[198:199], v232 offset:768\n"
"v_mfma_f32_16x16x32_bf16 v[4:7], a[136:139], v[128:131], v[4:7]\n"
"ds_read_b64_tr_b16 a[200:201], v233 offset:0\n"
"v_mfma_f32_16x16x32_bf16 v[4:7], a[124:127], v[124:127], v[4:7]\n"
"ds_read_b64_tr_b16 a[202:203], v233 offset:768\n"
"v_mfma_f32_16x16x32_bf16 v[4:7], a[112:115], v[120:123], v[4:7]\n"
"ds_read_b64_tr_b16 a[204:205], v231 offset:6144\n"
"v_mfma_f32_16x16x32_bf16 v[8:11], a[116:119], v[120:123], v[8:11]\n"
"ds_read_b64_tr_b16 a[206:207], v231 offset:6912\n"
"v_mfma_f32_16x16x32_bf16 v[8:11], a[128:131], v[124:127], v[8:11]\n"
"ds_read_b64_tr_b16 a[208:209], v232 offset:6144\n"
"v_mfma_f32_16x16x32_bf16 v[8:11], a[140:143], v[128:131], v[8:11]\n"
"ds_read_b64_tr_b16 a[210:211], v232 offset:6912\n"
"s_waitcnt lgkmcnt(7)\n"
"v_mfma_f32_16x16x32_bf16 v[0:3], a[144:147], v[108:111], v[0:3]\n"
"ds_read_b64_tr_b16 a[212:213], v233 offset:6144\n"
"v_mfma_f32_16x16x32_bf16 v[0:3], a[156:159], v[112:115], v[0:3]\n"
"ds_read_b64_tr_b16 a[214:215], v233 offset:6912\n"
"v_mfma_f32_16x16x32_bf16 v[0:3], a[168:171], v[116:119], v[0:3]\n"
"ds_read_b128 v[120:123], v212 offset:20864\n"
"v_mfma_f32_16x16x32_bf16 v[4:7], a[172:175], v[116:119], v[4:7]\n"
"ds_read_b128 v[124:127], v212 offset:20928\n"
"v_mfma_f32_16x16x32_bf16 v[4:7], a[160:163], v[112:115], v[4:7]\n"
"ds_read_b128 v[128:131], v212 offset:20992\n"
"v_mfma_f32_16x16x32_bf16 v[4:7], a[148:151], v[108:111], v[4:7]\n"
"v_mfma_f32_16x16x32_bf16 v[8:11], a[152:155], v[108:111], v[8:11]\n"
"v_mfma_f32_16x16x32_bf16 v[8:11], a[164:167], v[112:115], v[8:11]\n"
"v_mfma_f32_16x16x32_bf16 v[8:11], a[176:179], v[116:119], v[8:11]\n"
"v_mfma_f32_16x16x32_bf16 v[12:15], a[132:135], v[116:119], v[12:15]\n"
"v_mfma_f32_16x16x32_bf16 v[12:15], a[120:123], v[112:115], v[12:15]\n"
"v_mfma_f32_16x16x32_bf16 v[12:15], a[108:111], v[108:111], v[12:15]\n"
"v_mfma_f32_16x16x32_bf16 v[16:19], a[112:115], v[108:111], v[16:19]\n"
"v_mfma_f32_16x16x32_bf16 v[16:19], a[124:127], v[112:115], v[16:19]\n"
"v_mfma_f32_16x16x32_bf16 v[16:19], a[136:139], v[116:119], v[16:19]\n"
"v_mfma_f32_16x16x32_bf16 v[20:23], a[140:143], v[116:119], v[20:23]\n"
"v_mfma_f32_16x16x32_bf16 v[20:23], a[128:131], v[112:115], v[20:23]\n"
"v_mfma_f32_16x16x32_bf16 v[20:23], a[116:119], v[108:111], v[20:23]\n"
"ds_read_b128 v[108:111], v213 offset:21056\n"
"s_waitcnt lgkmcnt(1)\n"
"v_mfma_f32_16x16x32_bf16 v[0:3], a[180:183], v[120:123], v[0:3]\n"
"ds_read_b128 v[112:115], v213 offset:21120\n"
"v_mfma_f32_16x16x32_bf16 v[0:3], a[192:195], v[124:127], v[0:3]\n"
"ds_read_b128 v[116:119], v213 offset:21184\n"
"v_mfma_f32_16x16x32_bf16 v[0:3], a[204:207], v[128:131], v[0:3]\n"
"v_mfma_f32_16x16x32_bf16 v[4:7], a[208:211], v[128:131], v[4:7]\n"
"v_mfma_f32_16x16x32_bf16 v[4:7], a[196:199], v[124:127], v[4:7]\n"
"v_mfma_f32_16x16x32_bf16 v[4:7], a[184:187], v[120:123], v[4:7]\n"
"v_mfma_f32_16x16x32_bf16 v[8:11], a[188:191], v[120:123], v[8:11]\n"
"v_mfma_f32_16x16x32_bf16 v[8:11], a[200:203], v[124:127], v[8:11]\n"
"v_mfma_f32_16x16x32_bf16 v[8:11], a[212:215], v[128:131], v[8:11]\n"
"v_mfma_f32_16x16x32_bf16 v[12:15], a[168:171], v[128:131], v[12:15]\n"
"s_add_u32 m0, s46, 98304\n"
"s_add_u32 s47, s41, 24576\n"
"v_mfma_f32_16x16x32_bf16 v[12:15], a[156:159], v[124:127], v[12:15]\n"
"v_mfma_f32_16x16x32_bf16 v[12:15], a[144:147], v[120:123], v[12:15]\n"
"buffer_load_dwordx4 v228, s[20:23], s47 offen offset:0 lds\n"
"v_mfma_f32_16x16x32_bf16 v[16:19], a[148:151], v[120:123], v[16:19]\n"
"v_mfma_f32_16x16x32_bf16 v[16:19], a[160:163], v[124:127], v[16:19]\n"
"v_mfma_f32_16x16x32_bf16 v[16:19], a[172:175], v[128:131], v[16:19]\n"
"v_mfma_f32_16x16x32_bf16 v[20:23], a[176:179], v[128:131], v[20:23]\n"
"buffer_load_dwordx4 v229, s[20:23], s47 offen offset:1024 lds\n"
"v_mfma_f32_16x16x32_bf16 v[20:23], a[164:167], v[124:127], v[20:23]\n"
"v_mfma_f32_16x16x32_bf16 v[20:23], a[152:155], v[120:123], v[20:23]\n"
"v_mfma_f32_16x16x32_bf16 v[24:27], a[108:111], v[120:123], v[24:27]\n"
"v_mfma_f32_16x16x32_bf16 v[24:27], a[120:123], v[124:127], v[24:27]\n"
"buffer_load_dwordx4 v230, s[20:23], s47 offen offset:2048 lds\n"
"v_mfma_f32_16x16x32_bf16 v[24:27], a[132:135], v[128:131], v[24:27]\n"
"s_add_u32 m0, s46, 110592\n"
"s_add_u32 s47, s41, 36864\n"
"v_mfma_f32_16x16x32_bf16 v[28:31], a[136:139], v[128:131], v[28:31]\n"
"v_mfma_f32_16x16x32_bf16 v[28:31], a[124:127], v[124:127], v[28:31]\n"
"v_mfma_f32_16x16x32_bf16 v[28:31], a[112:115], v[120:123], v[28:31]\n"
"buffer_load_dwordx4 v228, s[20:23], s47 offen offset:0 lds\n"
"v_mfma_f32_16x16x32_bf16 v[32:35], a[116:119], v[120:123], v[32:35]\n"
"v_mfma_f32_16x16x32_bf16 v[32:35], a[128:131], v[124:127], v[32:35]\n"
"v_mfma_f32_16x16x32_bf16 v[32:35], a[140:143], v[128:131], v[32:35]\n"
"ds_read_b128 v[120:123], v213 offset:21248\n"
"s_waitcnt lgkmcnt(1)\n"
"v_mfma_f32_16x16x32_bf16 v[12:15], a[180:183], v[108:111], v[12:15]\n"
"buffer_load_dwordx4 v229, s[20:23], s47 offen offset:1024 lds\n"
"v_mfma_f32_16x16x32_bf16 v[12:15], a[192:195], v[112:115], v[12:15]\n"
"ds_read_b128 v[124:127], v213 offset:21312\n"
"v_mfma_f32_16x16x32_bf16 v[12:15], a[204:207], v[116:119], v[12:15]\n"
"ds_read_b128 v[128:131], v213 offset:21376\n"
"v_mfma_f32_16x16x32_bf16 v[16:19], a[208:211], v[116:119], v[16:19]\n"
"v_mfma_f32_16x16x32_bf16 v[16:19], a[196:199], v[112:115], v[16:19]\n"
"buffer_load_dwordx4 v230, s[20:23], s47 offen offset:2048 lds\n"
"v_mfma_f32_16x16x32_bf16 v[16:19], a[184:187], v[108:111], v[16:19]\n"
"s_add_u32 m0, s46, 122880\n"
"v_mfma_f32_16x16x32_bf16 v[20:23], a[188:191], v[108:111], v[20:23]\n"
"v_mfma_f32_16x16x32_bf16 v[20:23], a[200:203], v[112:115], v[20:23]\n"
"v_mfma_f32_16x16x32_bf16 v[20:23], a[212:215], v[116:119], v[20:23]\n"
"buffer_load_dwordx4 v228, s[20:23], s43 offen offset:0 lds\n"
"v_mfma_f32_16x16x32_bf16 v[24:27], a[168:171], v[116:119], v[24:27]\n"
"v_mfma_f32_16x16x32_bf16 v[24:27], a[156:159], v[112:115], v[24:27]\n"
"v_mfma_f32_16x16x32_bf16 v[24:27], a[144:147], v[108:111], v[24:27]\n"
"v_mfma_f32_16x16x32_bf16 v[28:31], a[148:151], v[108:111], v[28:31]\n"
"buffer_load_dwordx4 v229, s[20:23], s43 offen offset:1024 lds\n"
"v_mfma_f32_16x16x32_bf16 v[28:31], a[160:163], v[112:115], v[28:31]\n"
"s_waitcnt vmcnt(8) lgkmcnt(0)\n"
"s_barrier\n"
"v_mfma_f32_16x16x32_bf16 v[28:31], a[172:175], v[116:119], v[28:31]\n"
"ds_read_b64_tr_b16 a[216:217], v231 offset:12288\n"
"v_mfma_f32_16x16x32_bf16 v[32:35], a[176:179], v[116:119], v[32:35]\n"
"ds_read_b64_tr_b16 a[218:219], v231 offset:13056\n"
"v_mfma_f32_16x16x32_bf16 v[32:35], a[164:167], v[112:115], v[32:35]\n"
"buffer_load_dwordx4 v230, s[20:23], s43 offen offset:2048 lds\n"
"v_mfma_f32_16x16x32_bf16 v[32:35], a[152:155], v[108:111], v[32:35]\n"
"ds_read_b128 v[108:111], v212 offset:40960\n"
"v_mfma_f32_16x16x32_bf16 v[24:27], a[180:183], v[120:123], v[24:27]\n"
"ds_read_b128 v[112:115], v212 offset:41024\n"
"v_mfma_f32_16x16x32_bf16 v[24:27], a[192:195], v[124:127], v[24:27]\n"
"ds_read_b128 v[116:119], v212 offset:41088\n"
"v_mfma_f32_16x16x32_bf16 v[24:27], a[204:207], v[128:131], v[24:27]\n"
"ds_read_b64_tr_b16 a[220:221], v232 offset:12288\n"
"v_mfma_f32_16x16x32_bf16 v[28:31], a[208:211], v[128:131], v[28:31]\n"
"ds_read_b64_tr_b16 a[222:223], v232 offset:13056\n"
"v_mfma_f32_16x16x32_bf16 v[28:31], a[196:199], v[124:127], v[28:31]\n"
"ds_read_b64_tr_b16 a[224:225], v233 offset:12288\n"
"v_mfma_f32_16x16x32_bf16 v[28:31], a[184:187], v[120:123], v[28:31]\n"
"ds_read_b64_tr_b16 a[226:227], v233 offset:13056\n"
"v_mfma_f32_16x16x32_bf16 v[32:35], a[188:191], v[120:123], v[32:35]\n"
"ds_read_b64_tr_b16 a[228:229], v231 offset:18432\n"
"v_mfma_f32_16x16x32_bf16 v[32:35], a[200:203], v[124:127], v[32:35]\n"
"ds_read_b64_tr_b16 a[230:231], v231 offset:19200\n"
"v_mfma_f32_16x16x32_bf16 v[32:35], a[212:215], v[128:131], v[32:35]\n"
"ds_read_b128 v[120:123], v212 offset:41152\n"
"s_waitcnt lgkmcnt(7)\n"
"v_mfma_f32_16x16x32_bf16 v[36:39], a[108:111], v[108:111], v[36:39]\n"
"ds_read_b128 v[124:127], v212 offset:41216\n"
"s_add_u32 m0, s44, 0\n"
"v_mfma_f32_16x16x32_bf16 v[36:39], a[120:123], v[112:115], v[36:39]\n"
"ds_read_b128 v[128:131], v212 offset:41280\n"
"s_add_u32 s36, s36, s37\n"
"v_mfma_f32_16x16x32_bf16 v[36:39], a[132:135], v[116:119], v[36:39]\n"
"ds_read_b64_tr_b16 a[232:233], v232 offset:18432\n"
"v_mfma_f32_16x16x32_bf16 v[40:43], a[136:139], v[116:119], v[40:43]\n"
"buffer_load_dwordx4 v214, s[4:7], s36 offen offset:0 lds\n"
"v_mfma_f32_16x16x32_bf16 v[40:43], a[124:127], v[112:115], v[40:43]\n"
"ds_read_b64_tr_b16 a[234:235], v232 offset:19200\n"
"v_mfma_f32_16x16x32_bf16 v[40:43], a[112:115], v[108:111], v[40:43]\n"
"ds_read_b64_tr_b16 a[236:237], v233 offset:18432\n"
"v_mfma_f32_16x16x32_bf16 v[44:47], a[116:119], v[108:111], v[44:47]\n"
"ds_read_b64_tr_b16 a[238:239], v233 offset:19200\n"
"v_mfma_f32_16x16x32_bf16 v[44:47], a[128:131], v[112:115], v[44:47]\n"
"ds_read_b64_tr_b16 a[240:241], v231 offset:24576\n"
"v_mfma_f32_16x16x32_bf16 v[44:47], a[140:143], v[116:119], v[44:47]\n"
"ds_read_b64_tr_b16 a[242:243], v231 offset:25344\n"
"v_mfma_f32_16x16x32_bf16 v[72:75], a[24:27], v[116:119], v[132:135]\n"
"ds_read_b64_tr_b16 a[244:245], v232 offset:24576\n"
"v_mfma_f32_16x16x32_bf16 v[72:75], a[12:15], v[112:115], v[72:75]\n"
"ds_read_b64_tr_b16 a[246:247], v232 offset:25344\n"
"v_mfma_f32_16x16x32_bf16 v[72:75], a[0:3], v[108:111], v[72:75]\n"
"ds_read_b64_tr_b16 a[248:249], v233 offset:24576\n"
"v_mfma_f32_16x16x32_bf16 v[76:79], a[4:7], v[108:111], v[136:139]\n"
"ds_read_b64_tr_b16 a[250:251], v233 offset:25344\n"
"v_mfma_f32_16x16x32_bf16 v[76:79], a[16:19], v[112:115], v[76:79]\n"
"ds_read_b64_tr_b16 a[252:253], v231 offset:30720\n"
"v_mfma_f32_16x16x32_bf16 v[76:79], a[28:31], v[116:119], v[76:79]\n"
"ds_read_b64_tr_b16 a[254:255], v231 offset:31488\n"
"v_mfma_f32_16x16x32_bf16 v[80:83], a[32:35], v[116:119], v[140:143]\n"
"ds_read_b64_tr_b16 v[144:145], v232 offset:30720\n"
"v_mfma_f32_16x16x32_bf16 v[80:83], a[20:23], v[112:115], v[80:83]\n"
"ds_read_b64_tr_b16 v[146:147], v232 offset:31488\n"
"v_mfma_f32_16x16x32_bf16 v[80:83], a[8:11], v[108:111], v[80:83]\n"
"ds_read_b128 v[108:111], v212 offset:41344\n"
"s_waitcnt lgkmcnt(15)\n"
"v_mfma_f32_16x16x32_bf16 v[36:39], a[144:147], v[120:123], v[36:39]\n"
"buffer_load_dwordx4 v215, s[4:7], s36 offen offset:1024 lds\n"
"v_mfma_f32_16x16x32_bf16 v[36:39], a[156:159], v[124:127], v[36:39]\n"
"ds_read_b128 v[112:115], v212 offset:41408\n"
"v_mfma_f32_16x16x32_bf16 v[36:39], a[168:171], v[128:131], v[36:39]\n"
"ds_read_b128 v[116:119], v212 offset:41472\n"
"v_mfma_f32_16x16x32_bf16 v[40:43], a[172:175], v[128:131], v[40:43]\n"
"ds_read_b64_tr_b16 v[148:149], v233 offset:30720\n"
"v_mfma_f32_16x16x32_bf16 v[40:43], a[160:163], v[124:127], v[40:43]\n"
"ds_read_b64_tr_b16 v[150:151], v233 offset:31488\n"
"v_mfma_f32_16x16x32_bf16 v[40:43], a[148:151], v[120:123], v[40:43]\n"
"v_mfma_f32_16x16x32_bf16 v[44:47], a[152:155], v[120:123], v[44:47]\n"
"v_mfma_f32_16x16x32_bf16 v[44:47], a[164:167], v[124:127], v[44:47]\n"
"v_mfma_f32_16x16x32_bf16 v[44:47], a[176:179], v[128:131], v[44:47]\n"
"v_mfma_f32_16x16x32_bf16 v[48:51], a[132:135], v[128:131], v[48:51]\n"
"v_mfma_f32_16x16x32_bf16 v[48:51], a[120:123], v[124:127], v[48:51]\n"
"v_mfma_f32_16x16x32_bf16 v[48:51], a[108:111], v[120:123], v[48:51]\n"
"v_mfma_f32_16x16x32_bf16 v[52:55], a[112:115], v[120:123], v[52:55]\n"
"v_mfma_f32_16x16x32_bf16 v[52:55], a[124:127], v[124:127], v[52:55]\n"
"v_mfma_f32_16x16x32_bf16 v[52:55], a[136:139], v[128:131], v[52:55]\n"
"buffer_load_dwordx4 v216, s[4:7], s36 offen offset:2048 lds\n"
"v_mfma_f32_16x16x32_bf16 v[56:59], a[140:143], v[128:131], v[56:59]\n"
"v_mfma_f32_16x16x32_bf16 v[56:59], a[128:131], v[124:127], v[56:59]\n"
"v_mfma_f32_16x16x32_bf16 v[56:59], a[116:119], v[120:123], v[56:59]\n"
"v_mfma_f32_16x16x32_bf16 v[72:75], a[36:39], v[120:123], v[72:75]\n"
"v_mfma_f32_16x16x32_bf16 v[72:75], a[48:51], v[124:127], v[72:75]\n"
"v_mfma_f32_16x16x32_bf16 v[72:75], a[60:63], v[128:131], v[72:75]\n"
"v_mfma_f32_16x16x32_bf16 v[76:79], a[64:67], v[128:131], v[76:79]\n"
"v_mfma_f32_16x16x32_bf16 v[76:79], a[52:55], v[124:127], v[76:79]\n"
"v_mfma_f32_16x16x32_bf16 v[76:79], a[40:43], v[120:123], v[76:79]\n"
"v_mfma_f32_16x16x32_bf16 v[80:83], a[44:47], v[120:123], v[80:83]\n"
"v_mfma_f32_16x16x32_bf16 v[80:83], a[56:59], v[124:127], v[80:83]\n"
"v_mfma_f32_16x16x32_bf16 v[80:83], a[68:71], v[128:131], v[80:83]\n"
"v_mfma_f32_16x16x32_bf16 v[84:87], a[24:27], v[128:131], v[132:135]\n"
"v_mfma_f32_16x16x32_bf16 v[84:87], a[12:15], v[124:127], v[84:87]\n"
"buffer_load_dwordx4 v217, s[4:7], s36 offen offset:3072 lds\n"
"v_mfma_f32_16x16x32_bf16 v[84:87], a[0:3], v[120:123], v[84:87]\n"
"v_mfma_f32_16x16x32_bf16 v[88:91], a[4:7], v[120:123], v[136:139]\n"
"v_mfma_f32_16x16x32_bf16 v[88:91], a[16:19], v[124:127], v[88:91]\n"
"v_mfma_f32_16x16x32_bf16 v[88:91], a[28:31], v[128:131], v[88:91]\n"
"v_mfma_f32_16x16x32_bf16 v[92:95], a[32:35], v[128:131], v[140:143]\n"
"v_mfma_f32_16x16x32_bf16 v[92:95], a[20:23], v[124:127], v[92:95]\n"
"v_mfma_f32_16x16x32_bf16 v[92:95], a[8:11], v[120:123], v[92:95]\n"
"ds_read_b128 v[120:123], v213 offset:41536\n"
"s_waitcnt lgkmcnt(3)\n"
"v_mfma_f32_16x16x32_bf16 v[36:39], a[180:183], v[108:111], v[36:39]\n"
"ds_read_b128 v[124:127], v213 offset:41600\n"
"v_mfma_f32_16x16x32_bf16 v[36:39], a[192:195], v[112:115], v[36:39]\n"
"ds_read_b128 v[128:131], v213 offset:41664\n"
"v_mfma_f32_16x16x32_bf16 v[36:39], a[204:207], v[116:119], v[36:39]\n"
"v_mfma_f32_16x16x32_bf16 v[40:43], a[208:211], v[116:119], v[40:43]\n"
"s_add_u32 m0, s45, 16384\n"
"v_mfma_f32_16x16x32_bf16 v[40:43], a[196:199], v[112:115], v[40:43]\n"
"v_mfma_f32_16x16x32_bf16 v[40:43], a[184:187], v[108:111], v[40:43]\n"
"v_mfma_f32_16x16x32_bf16 v[44:47], a[188:191], v[108:111], v[44:47]\n"
"buffer_load_dwordx4 v218, s[4:7], s36 offen lds\n"
"v_mfma_f32_16x16x32_bf16 v[44:47], a[200:203], v[112:115], v[44:47]\n"
"v_mfma_f32_16x16x32_bf16 v[44:47], a[212:215], v[116:119], v[44:47]\n"
"v_mfma_f32_16x16x32_bf16 v[48:51], a[168:171], v[116:119], v[48:51]\n"
"v_mfma_f32_16x16x32_bf16 v[48:51], a[156:159], v[112:115], v[48:51]\n"
"v_mfma_f32_16x16x32_bf16 v[48:51], a[144:147], v[108:111], v[48:51]\n"
"v_mfma_f32_16x16x32_bf16 v[52:55], a[148:151], v[108:111], v[52:55]\n"
"v_mfma_f32_16x16x32_bf16 v[52:55], a[160:163], v[112:115], v[52:55]\n"
"v_mfma_f32_16x16x32_bf16 v[52:55], a[172:175], v[116:119], v[52:55]\n"
"v_mfma_f32_16x16x32_bf16 v[56:59], a[176:179], v[116:119], v[56:59]\n"
"v_mfma_f32_16x16x32_bf16 v[56:59], a[164:167], v[112:115], v[56:59]\n"
"v_mfma_f32_16x16x32_bf16 v[56:59], a[152:155], v[108:111], v[56:59]\n"
"v_mfma_f32_16x16x32_bf16 v[60:63], a[108:111], v[108:111], v[60:63]\n"
"v_mfma_f32_16x16x32_bf16 v[60:63], a[120:123], v[112:115], v[60:63]\n"
"s_add_u32 m0, s44, 143360\n"
"s_add_u32 s36, s36, s37\n"
"v_mfma_f32_16x16x32_bf16 v[60:63], a[132:135], v[116:119], v[60:63]\n"
"v_mfma_f32_16x16x32_bf16 v[64:67], a[136:139], v[116:119], v[64:67]\n"
"v_mfma_f32_16x16x32_bf16 v[64:67], a[124:127], v[112:115], v[64:67]\n"
"buffer_load_dwordx4 v214, s[4:7], s36 offen offset:0 lds\n"
"v_mfma_f32_16x16x32_bf16 v[64:67], a[112:115], v[108:111], v[64:67]\n"
"v_mfma_f32_16x16x32_bf16 v[68:71], a[116:119], v[108:111], v[68:71]\n"
"v_mfma_f32_16x16x32_bf16 v[68:71], a[128:131], v[112:115], v[68:71]\n"
"v_mfma_f32_16x16x32_bf16 v[68:71], a[140:143], v[116:119], v[68:71]\n"
"v_mfma_f32_16x16x32_bf16 v[72:75], a[96:99], v[116:119], v[72:75]\n"
"v_mfma_f32_16x16x32_bf16 v[72:75], a[84:87], v[112:115], v[72:75]\n"
"v_mfma_f32_16x16x32_bf16 v[72:75], a[72:75], v[108:111], v[72:75]\n"
"v_mfma_f32_16x16x32_bf16 v[76:79], a[76:79], v[108:111], v[76:79]\n"
"v_mfma_f32_16x16x32_bf16 v[76:79], a[88:91], v[112:115], v[76:79]\n"
"v_mfma_f32_16x16x32_bf16 v[76:79], a[100:103], v[116:119], v[76:79]\n"
"v_mfma_f32_16x16x32_bf16 v[80:83], a[104:107], v[116:119], v[80:83]\n"
"v_mfma_f32_16x16x32_bf16 v[80:83], a[92:95], v[112:115], v[80:83]\n"
"v_mfma_f32_16x16x32_bf16 v[80:83], a[80:83], v[108:111], v[80:83]\n"
"v_mfma_f32_16x16x32_bf16 v[84:87], a[36:39], v[108:111], v[84:87]\n"
"v_mfma_f32_16x16x32_bf16 v[84:87], a[48:51], v[112:115], v[84:87]\n"
"v_mfma_f32_16x16x32_bf16 v[84:87], a[60:63], v[116:119], v[84:87]\n"
"buffer_load_dwordx4 v215, s[4:7], s36 offen offset:1024 lds\n"
"v_mfma_f32_16x16x32_bf16 v[88:91], a[64:67], v[116:119], v[88:91]\n"
"v_mfma_f32_16x16x32_bf16 v[88:91], a[52:55], v[112:115], v[88:91]\n"
"v_mfma_f32_16x16x32_bf16 v[88:91], a[40:43], v[108:111], v[88:91]\n"
"v_mfma_f32_16x16x32_bf16 v[92:95], a[44:47], v[108:111], v[92:95]\n"
"v_mfma_f32_16x16x32_bf16 v[92:95], a[56:59], v[112:115], v[92:95]\n"
"s_waitcnt vmcnt(7) lgkmcnt(0)\n"
"s_barrier\n"
"v_mfma_f32_16x16x32_bf16 v[92:95], a[68:71], v[116:119], v[92:95]\n"
"ds_read_b64_tr_b16 v[152:153], v231 offset:36864\n"
"v_mfma_f32_16x16x32_bf16 v[96:99], a[24:27], v[116:119], v[132:135]\n"
"ds_read_b64_tr_b16 v[154:155], v231 offset:37632\n"
"v_mfma_f32_16x16x32_bf16 v[96:99], a[12:15], v[112:115], v[96:99]\n"
"ds_read_b64_tr_b16 v[156:157], v232 offset:36864\n"
"v_mfma_f32_16x16x32_bf16 v[96:99], a[0:3], v[108:111], v[96:99]\n"
"ds_read_b64_tr_b16 v[158:159], v232 offset:37632\n"
"v_mfma_f32_16x16x32_bf16 v[100:103], a[4:7], v[108:111], v[136:139]\n"
"ds_read_b64_tr_b16 v[160:161], v233 offset:36864\n"
"v_mfma_f32_16x16x32_bf16 v[100:103], a[16:19], v[112:115], v[100:103]\n"
"ds_read_b64_tr_b16 v[162:163], v233 offset:37632\n"
"v_mfma_f32_16x16x32_bf16 v[100:103], a[28:31], v[116:119], v[100:103]\n"
"ds_read_b64_tr_b16 v[164:165], v231 offset:43008\n"
"v_mfma_f32_16x16x32_bf16 v[104:107], a[32:35], v[116:119], v[140:143]\n"
"ds_read_b64_tr_b16 v[166:167], v231 offset:43776\n"
"v_mfma_f32_16x16x32_bf16 v[104:107], a[20:23], v[112:115], v[104:107]\n"
"ds_read_b64_tr_b16 v[168:169], v232 offset:43008\n"
"v_mfma_f32_16x16x32_bf16 v[104:107], a[8:11], v[108:111], v[104:107]\n"
"ds_read_b128 v[108:111], v213 offset:41728\n"
"v_mfma_f32_16x16x32_bf16 v[48:51], a[180:183], v[120:123], v[48:51]\n"
"buffer_load_dwordx4 v216, s[4:7], s36 offen offset:2048 lds\n"
"v_mfma_f32_16x16x32_bf16 v[48:51], a[192:195], v[124:127], v[48:51]\n"
"ds_read_b128 v[112:115], v213 offset:41792\n"
"v_mfma_f32_16x16x32_bf16 v[48:51], a[204:207], v[128:131], v[48:51]\n"
"ds_read_b128 v[116:119], v213 offset:41856\n"
"v_mfma_f32_16x16x32_bf16 v[52:55], a[208:211], v[128:131], v[52:55]\n"
"ds_read_b64_tr_b16 v[170:171], v232 offset:43776\n"
"v_mfma_f32_16x16x32_bf16 v[52:55], a[196:199], v[124:127], v[52:55]\n"
"ds_read_b64_tr_b16 v[172:173], v233 offset:43008\n"
"v_mfma_f32_16x16x32_bf16 v[52:55], a[184:187], v[120:123], v[52:55]\n"
"ds_read_b64_tr_b16 v[174:175], v233 offset:43776\n"
"v_mfma_f32_16x16x32_bf16 v[56:59], a[188:191], v[120:123], v[56:59]\n"
"ds_read_b64_tr_b16 v[176:177], v231 offset:49152\n"
"v_mfma_f32_16x16x32_bf16 v[56:59], a[200:203], v[124:127], v[56:59]\n"
"ds_read_b64_tr_b16 v[178:179], v231 offset:49920\n"
"v_mfma_f32_16x16x32_bf16 v[56:59], a[212:215], v[128:131], v[56:59]\n"
"ds_read_b64_tr_b16 v[180:181], v232 offset:49152\n"
"v_mfma_f32_16x16x32_bf16 v[60:63], a[168:171], v[128:131], v[60:63]\n"
"ds_read_b64_tr_b16 v[182:183], v232 offset:49920\n"
"v_mfma_f32_16x16x32_bf16 v[60:63], a[156:159], v[124:127], v[60:63]\n"
"ds_read_b64_tr_b16 v[184:185], v233 offset:49152\n"
"v_mfma_f32_16x16x32_bf16 v[60:63], a[144:147], v[120:123], v[60:63]\n"
"ds_read_b64_tr_b16 v[186:187], v233 offset:49920\n"
"v_mfma_f32_16x16x32_bf16 v[64:67], a[148:151], v[120:123], v[64:67]\n"
"ds_read_b64_tr_b16 v[188:189], v231 offset:55296\n"
"v_mfma_f32_16x16x32_bf16 v[64:67], a[160:163], v[124:127], v[64:67]\n"
"ds_read_b64_tr_b16 v[190:191], v231 offset:56064\n"
"v_mfma_f32_16x16x32_bf16 v[64:67], a[172:175], v[128:131], v[64:67]\n"
"ds_read_b64_tr_b16 v[192:193], v232 offset:55296\n"
"v_mfma_f32_16x16x32_bf16 v[68:71], a[176:179], v[128:131], v[68:71]\n"
"ds_read_b64_tr_b16 v[194:195], v232 offset:56064\n"
"v_mfma_f32_16x16x32_bf16 v[68:71], a[164:167], v[124:127], v[68:71]\n"
"buffer_load_dwordx4 v217, s[4:7], s36 offen offset:3072 lds\n"
"v_mfma_f32_16x16x32_bf16 v[68:71], a[152:155], v[120:123], v[68:71]\n"
"ds_read_b64_tr_b16 v[196:197], v233 offset:55296\n"
"v_mfma_f32_16x16x32_bf16 v[84:87], a[72:75], v[120:123], v[84:87]\n"
"ds_read_b64_tr_b16 v[198:199], v233 offset:56064\n"
"v_mfma_f32_16x16x32_bf16 v[84:87], a[84:87], v[124:127], v[84:87]\n"
"ds_read_b64_tr_b16 v[200:201], v231 offset:61440\n"
"v_mfma_f32_16x16x32_bf16 v[84:87], a[96:99], v[128:131], v[84:87]\n"
"ds_read_b64_tr_b16 v[202:203], v231 offset:62208\n"
"v_mfma_f32_16x16x32_bf16 v[88:91], a[100:103], v[128:131], v[88:91]\n"
"ds_read_b64_tr_b16 v[204:205], v232 offset:61440\n"
"v_mfma_f32_16x16x32_bf16 v[88:91], a[88:91], v[124:127], v[88:91]\n"
"ds_read_b64_tr_b16 v[206:207], v232 offset:62208\n"
"v_mfma_f32_16x16x32_bf16 v[88:91], a[76:79], v[120:123], v[88:91]\n"
"ds_read_b64_tr_b16 v[208:209], v233 offset:61440\n"
"v_mfma_f32_16x16x32_bf16 v[92:95], a[80:83], v[120:123], v[92:95]\n"
"ds_read_b64_tr_b16 v[210:211], v233 offset:62208\n"
"v_mfma_f32_16x16x32_bf16 v[92:95], a[92:95], v[124:127], v[92:95]\n"
"v_mfma_f32_16x16x32_bf16 v[92:95], a[104:107], v[128:131], v[92:95]\n"
"v_mfma_f32_16x16x32_bf16 v[96:99], a[60:63], v[128:131], v[96:99]\n"
"v_mfma_f32_16x16x32_bf16 v[96:99], a[48:51], v[124:127], v[96:99]\n"
"v_mfma_f32_16x16x32_bf16 v[96:99], a[36:39], v[120:123], v[96:99]\n"
"s_add_u32 m0, s45, 159744\n"
"v_mfma_f32_16x16x32_bf16 v[100:103], a[40:43], v[120:123], v[100:103]\n"
"v_mfma_f32_16x16x32_bf16 v[100:103], a[52:55], v[124:127], v[100:103]\n"
"v_mfma_f32_16x16x32_bf16 v[100:103], a[64:67], v[128:131], v[100:103]\n"
"buffer_load_dwordx4 v218, s[4:7], s36 offen lds\n"
"v_mfma_f32_16x16x32_bf16 v[104:107], a[68:71], v[128:131], v[104:107]\n"
"v_mfma_f32_16x16x32_bf16 v[104:107], a[56:59], v[124:127], v[104:107]\n"
"v_mfma_f32_16x16x32_bf16 v[104:107], a[44:47], v[120:123], v[104:107]\n"
"ds_read_b128 v[120:123], v212 offset:40960\n"
"s_waitcnt lgkmcnt(15)\n"
"v_mfma_f32_16x16x32_bf16 v[60:63], a[180:183], v[108:111], v[60:63]\n"
"ds_read_b128 v[124:127], v212 offset:41024\n"
"v_mfma_f32_16x16x32_bf16 v[60:63], a[192:195], v[112:115], v[60:63]\n"
"ds_read_b128 v[128:131], v212 offset:41088\n"
"v_mfma_f32_16x16x32_bf16 v[60:63], a[204:207], v[116:119], v[60:63]\n"
"v_mfma_f32_16x16x32_bf16 v[64:67], a[208:211], v[116:119], v[64:67]\n"
"v_mfma_f32_16x16x32_bf16 v[64:67], a[196:199], v[112:115], v[64:67]\n"
"v_mfma_f32_16x16x32_bf16 v[64:67], a[184:187], v[108:111], v[64:67]\n"
"v_mfma_f32_16x16x32_bf16 v[68:71], a[188:191], v[108:111], v[68:71]\n"
"v_mfma_f32_16x16x32_bf16 v[68:71], a[200:203], v[112:115], v[68:71]\n"
"v_mfma_f32_16x16x32_bf16 v[68:71], a[212:215], v[116:119], v[68:71]\n"
"v_mfma_f32_16x16x32_bf16 v[96:99], a[96:99], v[116:119], v[96:99]\n"
"v_mfma_f32_16x16x32_bf16 v[96:99], a[84:87], v[112:115], v[96:99]\n"
"v_mfma_f32_16x16x32_bf16 v[96:99], a[72:75], v[108:111], v[96:99]\n"
"v_mfma_f32_16x16x32_bf16 v[100:103], a[76:79], v[108:111], v[100:103]\n"
"v_mfma_f32_16x16x32_bf16 v[100:103], a[88:91], v[112:115], v[100:103]\n"
"s_add_u32 m0, s44, 20480\n"
"s_add_u32 s36, s36, s37\n"
"v_mfma_f32_16x16x32_bf16 v[100:103], a[100:103], v[116:119], v[100:103]\n"
"v_mfma_f32_16x16x32_bf16 v[104:107], a[104:107], v[116:119], v[104:107]\n"
"v_mfma_f32_16x16x32_bf16 v[104:107], a[92:95], v[112:115], v[104:107]\n"
"buffer_load_dwordx4 v214, s[4:7], s36 offen offset:0 lds\n"
"v_mfma_f32_16x16x32_bf16 v[104:107], a[80:83], v[108:111], v[104:107]\n"
"ds_read_b128 v[108:111], v212 offset:41152\n"
"s_waitcnt lgkmcnt(1)\n"
"v_mfma_f32_16x16x32_bf16 v[0:3], a[216:219], v[120:123], v[0:3]\n"
"ds_read_b128 v[112:115], v212 offset:41216\n"
"v_mfma_f32_16x16x32_bf16 v[0:3], a[228:231], v[124:127], v[0:3]\n"
"ds_read_b128 v[116:119], v212 offset:41280\n"
"v_mfma_f32_16x16x32_bf16 v[0:3], a[240:243], v[128:131], v[0:3]\n"
"v_mfma_f32_16x16x32_bf16 v[4:7], a[244:247], v[128:131], v[4:7]\n"
"v_mfma_f32_16x16x32_bf16 v[4:7], a[232:235], v[124:127], v[4:7]\n"
"v_mfma_f32_16x16x32_bf16 v[4:7], a[220:223], v[120:123], v[4:7]\n"
"v_mfma_f32_16x16x32_bf16 v[8:11], a[224:227], v[120:123], v[8:11]\n"
"v_mfma_f32_16x16x32_bf16 v[8:11], a[236:239], v[124:127], v[8:11]\n"
"v_mfma_f32_16x16x32_bf16 v[8:11], a[248:251], v[128:131], v[8:11]\n"
"ds_read_b128 v[120:123], v212 offset:41344\n"
"s_waitcnt lgkmcnt(1)\n"
"v_mfma_f32_16x16x32_bf16 v[0:3], a[252:255], v[108:111], v[0:3]\n"
"ds_read_b128 v[124:127], v212 offset:41408\n"
"v_mfma_f32_16x16x32_bf16 v[0:3], v[152:155], v[112:115], v[0:3]\n"
"ds_read_b128 v[128:131], v212 offset:41472\n"
"v_mfma_f32_16x16x32_bf16 v[0:3], v[164:167], v[116:119], v[0:3]\n"
"v_mfma_f32_16x16x32_bf16 v[4:7], v[168:171], v[116:119], v[4:7]\n"
"v_mfma_f32_16x16x32_bf16 v[4:7], v[156:159], v[112:115], v[4:7]\n"
"v_mfma_f32_16x16x32_bf16 v[4:7], v[144:147], v[108:111], v[4:7]\n"
"v_mfma_f32_16x16x32_bf16 v[8:11], v[148:151], v[108:111], v[8:11]\n"
"v_mfma_f32_16x16x32_bf16 v[8:11], v[160:163], v[112:115], v[8:11]\n"
"v_mfma_f32_16x16x32_bf16 v[8:11], v[172:175], v[116:119], v[8:11]\n"
"v_mfma_f32_16x16x32_bf16 v[12:15], a[240:243], v[116:119], v[12:15]\n"
"buffer_load_dwordx4 v215, s[4:7], s36 offen offset:1024 lds\n"
"v_mfma_f32_16x16x32_bf16 v[12:15], a[228:231], v[112:115], v[12:15]\n"
"v_mfma_f32_16x16x32_bf16 v[12:15], a[216:219], v[108:111], v[12:15]\n"
"v_mfma_f32_16x16x32_bf16 v[16:19], a[220:223], v[108:111], v[16:19]\n"
"v_mfma_f32_16x16x32_bf16 v[16:19], a[232:235], v[112:115], v[16:19]\n"
"v_mfma_f32_16x16x32_bf16 v[16:19], a[244:247], v[116:119], v[16:19]\n"
"v_mfma_f32_16x16x32_bf16 v[20:23], a[248:251], v[116:119], v[20:23]\n"
"v_mfma_f32_16x16x32_bf16 v[20:23], a[236:239], v[112:115], v[20:23]\n"
"v_mfma_f32_16x16x32_bf16 v[20:23], a[224:227], v[108:111], v[20:23]\n"
"ds_read_b128 v[108:111], v213 offset:41536\n"
"s_waitcnt lgkmcnt(1)\n"
"v_mfma_f32_16x16x32_bf16 v[0:3], v[176:179], v[120:123], v[0:3]\n"
"ds_read_b128 v[112:115], v213 offset:41600\n"
"v_mfma_f32_16x16x32_bf16 v[0:3], v[188:191], v[124:127], v[0:3]\n"
"ds_read_b128 v[116:119], v213 offset:41664\n"
"v_mfma_f32_16x16x32_bf16 v[0:3], v[200:203], v[128:131], v[0:3]\n"
"v_mfma_f32_16x16x32_bf16 v[4:7], v[204:207], v[128:131], v[4:7]\n"
"v_mfma_f32_16x16x32_bf16 v[4:7], v[192:195], v[124:127], v[4:7]\n"
"v_mfma_f32_16x16x32_bf16 v[4:7], v[180:183], v[120:123], v[4:7]\n"
"v_mfma_f32_16x16x32_bf16 v[8:11], v[184:187], v[120:123], v[8:11]\n"
"v_mfma_f32_16x16x32_bf16 v[8:11], v[196:199], v[124:127], v[8:11]\n"
"v_mfma_f32_16x16x32_bf16 v[8:11], v[208:211], v[128:131], v[8:11]\n"
"v_mfma_f32_16x16x32_bf16 v[12:15], v[164:167], v[128:131], v[12:15]\n"
"v_mfma_f32_16x16x32_bf16 v[12:15], v[152:155], v[124:127], v[12:15]\n"
"v_cvt_pk_bf16_f32 v0, v0, v1\n"
"v_cvt_pk_bf16_f32 v1, v2, v3\n"
"v_mfma_f32_16x16x32_bf16 v[12:15], a[252:255], v[120:123], v[12:15]\n"
"buffer_load_dwordx4 v216, s[4:7], s36 offen offset:2048 lds\n"
"v_mfma_f32_16x16x32_bf16 v[16:19], v[144:147], v[120:123], v[16:19]\n"
"ds_write_b32 v219, v0 offset:0\n"
"v_mfma_f32_16x16x32_bf16 v[16:19], v[156:159], v[124:127], v[16:19]\n"
"v_mfma_f32_16x16x32_bf16 v[16:19], v[168:171], v[128:131], v[16:19]\n"
"ds_write_b32 v219, v1 offset:4\n"
"v_mfma_f32_16x16x32_bf16 v[20:23], v[172:175], v[128:131], v[20:23]\n"
"v_cvt_pk_bf16_f32 v4, v4, v5\n"
"v_cvt_pk_bf16_f32 v5, v6, v7\n"
"v_mfma_f32_16x16x32_bf16 v[20:23], v[160:163], v[124:127], v[20:23]\n"
"ds_write_b32 v219, v4 offset:32\n"
"v_mfma_f32_16x16x32_bf16 v[20:23], v[148:151], v[120:123], v[20:23]\n"
"v_mfma_f32_16x16x32_bf16 v[24:27], a[216:219], v[120:123], v[24:27]\n"
"ds_write_b32 v219, v5 offset:36\n"
"v_mfma_f32_16x16x32_bf16 v[24:27], a[228:231], v[124:127], v[24:27]\n"
"v_cvt_pk_bf16_f32 v8, v8, v9\n"
"v_cvt_pk_bf16_f32 v9, v10, v11\n"
"v_mfma_f32_16x16x32_bf16 v[24:27], a[240:243], v[128:131], v[24:27]\n"
"ds_write_b32 v219, v8 offset:64\n"
"v_mfma_f32_16x16x32_bf16 v[28:31], a[244:247], v[128:131], v[28:31]\n"
"v_mfma_f32_16x16x32_bf16 v[28:31], a[232:235], v[124:127], v[28:31]\n"
"ds_write_b32 v219, v9 offset:68\n"
"v_mfma_f32_16x16x32_bf16 v[28:31], a[220:223], v[120:123], v[28:31]\n"
"v_mfma_f32_16x16x32_bf16 v[32:35], a[224:227], v[120:123], v[32:35]\n"
"v_mfma_f32_16x16x32_bf16 v[32:35], a[236:239], v[124:127], v[32:35]\n"
"v_mfma_f32_16x16x32_bf16 v[32:35], a[248:251], v[128:131], v[32:35]\n"
"ds_read_b128 v[120:123], v213 offset:41728\n"
"s_waitcnt lgkmcnt(7)\n"
"v_mfma_f32_16x16x32_bf16 v[12:15], v[176:179], v[108:111], v[12:15]\n"
"ds_read_b128 v[124:127], v213 offset:41792\n"
"v_mfma_f32_16x16x32_bf16 v[12:15], v[188:191], v[112:115], v[12:15]\n"
"ds_read_b128 v[128:131], v213 offset:41856\n"
"v_mfma_f32_16x16x32_bf16 v[12:15], v[200:203], v[116:119], v[12:15]\n"
"v_mfma_f32_16x16x32_bf16 v[16:19], v[204:207], v[116:119], v[16:19]\n"
"s_waitcnt vmcnt(8)\n"
"s_barrier\n"
"v_mfma_f32_16x16x32_bf16 v[16:19], v[192:195], v[112:115], v[16:19]\n"
"buffer_load_dwordx4 v217, s[4:7], s36 offen offset:3072 lds\n"
"v_mfma_f32_16x16x32_bf16 v[16:19], v[180:183], v[108:111], v[16:19]\n"
"v_mfma_f32_16x16x32_bf16 v[20:23], v[184:187], v[108:111], v[20:23]\n"
"v_mfma_f32_16x16x32_bf16 v[20:23], v[196:199], v[112:115], v[20:23]\n"
"v_mfma_f32_16x16x32_bf16 v[20:23], v[208:211], v[116:119], v[20:23]\n"
"v_mfma_f32_16x16x32_bf16 v[24:27], v[164:167], v[116:119], v[24:27]\n"
"v_mfma_f32_16x16x32_bf16 v[24:27], v[152:155], v[112:115], v[24:27]\n"
"v_cvt_pk_bf16_f32 v12, v12, v13\n"
"v_cvt_pk_bf16_f32 v13, v14, v15\n"
"v_mfma_f32_16x16x32_bf16 v[24:27], a[252:255], v[108:111], v[24:27]\n"
"ds_write_b32 v219, v12 offset:192\n"
"v_mfma_f32_16x16x32_bf16 v[28:31], v[144:147], v[108:111], v[28:31]\n"
"v_mfma_f32_16x16x32_bf16 v[28:31], v[156:159], v[112:115], v[28:31]\n"
"ds_write_b32 v219, v13 offset:196\n"
"v_mfma_f32_16x16x32_bf16 v[28:31], v[168:171], v[116:119], v[28:31]\n"
"v_cvt_pk_bf16_f32 v16, v16, v17\n"
"v_cvt_pk_bf16_f32 v17, v18, v19\n"
"v_mfma_f32_16x16x32_bf16 v[32:35], v[172:175], v[116:119], v[32:35]\n"
"ds_write_b32 v219, v16 offset:224\n"
"v_mfma_f32_16x16x32_bf16 v[32:35], v[160:163], v[112:115], v[32:35]\n"
"v_mfma_f32_16x16x32_bf16 v[32:35], v[148:151], v[108:111], v[32:35]\n"
"ds_read_b128 v[108:111], v212 offset:0\n"
"s_waitcnt lgkmcnt(4)\n"
"v_mfma_f32_16x16x32_bf16 v[24:27], v[176:179], v[120:123], v[24:27]\n"
"ds_read_b128 v[112:115], v212 offset:64\n"
"v_mfma_f32_16x16x32_bf16 v[24:27], v[188:191], v[124:127], v[24:27]\n"
"ds_read_b128 v[116:119], v212 offset:128\n"
"v_mfma_f32_16x16x32_bf16 v[24:27], v[200:203], v[128:131], v[24:27]\n"
"ds_write_b32 v219, v17 offset:228\n"
"v_mfma_f32_16x16x32_bf16 v[28:31], v[204:207], v[128:131], v[28:31]\n"
"s_add_u32 m0, s45, 36864\n"
"v_cvt_pk_bf16_f32 v20, v20, v21\n"
"v_cvt_pk_bf16_f32 v21, v22, v23\n"
".Lconv_stub1:\n"
"s_cbranch_vccnz .Lconv_ret\n"
"v_mfma_f32_16x16x32_bf16 v[28:31], v[192:195], v[124:127], v[28:31]\n"
"ds_write_b32 v219, v20 offset:256\n"
"v_mfma_f32_16x16x32_bf16 v[28:31], v[180:183], v[120:123], v[28:31]\n"
"v_mfma_f32_16x16x32_bf16 v[32:35], v[184:187], v[120:123], v[32:35]\n"
"buffer_load_dwordx4 v218, s[4:7], s36 offen lds\n"
"v_mfma_f32_16x16x32_bf16 v[32:35], v[196:199], v[124:127], v[32:35]\n"
"ds_write_b32 v219, v21 offset:260\n"
"v_mfma_f32_16x16x32_bf16 v[32:35], v[208:211], v[128:131], v[32:35]\n"
"ds_read_b128 v[120:123], v212 offset:192\n"
"s_waitcnt lgkmcnt(4)\n"
"v_mfma_f32_16x16x32_bf16 v[36:39], a[216:219], v[108:111], v[36:39]\n"
"ds_read_b128 v[124:127], v212 offset:256\n"
"v_mfma_f32_16x16x32_bf16 v[36:39], a[228:231], v[112:115], v[36:39]\n"
"ds_read_b128 v[128:131], v212 offset:320\n"
"v_cvt_pk_bf16_f32 v24, v24, v25\n"
"v_mfma_f32_16x16x32_bf16 v[36:39], a[240:243], v[116:119], v[36:39]\n"
"v_cvt_pk_bf16_f32 v25, v26, v27\n"
"v_mfma_f32_16x16x32_bf16 v[40:43], a[244:247], v[116:119], v[40:43]\n"
"ds_write_b32 v219, v24 offset:384\n"
"v_mfma_f32_16x16x32_bf16 v[40:43], a[232:235], v[112:115], v[40:43]\n"
"v_mfma_f32_16x16x32_bf16 v[40:43], a[220:223], v[108:111], v[40:43]\n"
"ds_write_b32 v219, v25 offset:388\n"
"v_mfma_f32_16x16x32_bf16 v[44:47], a[224:227], v[108:111], v[44:47]\n"
"v_cvt_pk_bf16_f32 v28, v28, v29\n"
"v_cvt_pk_bf16_f32 v29, v30, v31\n"
"v_mfma_f32_16x16x32_bf16 v[44:47], a[236:239], v[112:115], v[44:47]\n"
"ds_write_b32 v219, v28 offset:416\n"
"v_mfma_f32_16x16x32_bf16 v[44:47], a[248:251], v[116:119], v[44:47]\n"
"v_mfma_f32_16x16x32_bf16 v[72:75], a[132:135], v[116:119], v[72:75]\n"
"ds_write_b32 v219, v29 offset:420\n"
"v_mfma_f32_16x16x32_bf16 v[72:75], a[120:123], v[112:115], v[72:75]\n"
"v_cvt_pk_bf16_f32 v32, v32, v33\n"
"v_cvt_pk_bf16_f32 v33, v34, v35\n"
"v_mfma_f32_16x16x32_bf16 v[72:75], a[108:111], v[108:111], v[72:75]\n"
"ds_write_b32 v219, v32 offset:448\n"
"v_mfma_f32_16x16x32_bf16 v[76:79], a[112:115], v[108:111], v[76:79]\n"
"v_mfma_f32_16x16x32_bf16 v[76:79], a[124:127], v[112:115], v[76:79]\n"
"ds_write_b32 v219, v33 offset:452\n"
"v_mfma_f32_16x16x32_bf16 v[76:79], a[136:139], v[116:119], v[76:79]\n"
"v_mfma_f32_16x16x32_bf16 v[80:83], a[140:143], v[116:119], v[80:83]\n"
"v_mfma_f32_16x16x32_bf16 v[80:83], a[128:131], v[112:115], v[80:83]\n"
"v_mfma_f32_16x16x32_bf16 v[80:83], a[116:119], v[108:111], v[80:83]\n"
"v_mfma_f32_16x16x32_bf16 v[0:3], a[0:3], v[108:111], v[132:135]\n"
"v_mfma_f32_16x16x32_bf16 v[0:3], a[12:15], v[112:115], v[0:3]\n"
"s_waitcnt vmcnt(5) lgkmcnt(0)\n"
"s_barrier\n"
"v_mfma_f32_16x16x32_bf16 v[0:3], a[24:27], v[116:119], v[0:3]\n"
"ds_read_b32 v228, v220 offset:0\n"
"v_mfma_f32_16x16x32_bf16 v[4:7], a[28:31], v[116:119], v[136:139]\n"
"ds_read_b32 v229, v220 offset:256\n"
"v_mfma_f32_16x16x32_bf16 v[4:7], a[16:19], v[112:115], v[4:7]\n"
"ds_read_b32 v230, v221 offset:512\n"
"v_mfma_f32_16x16x32_bf16 v[4:7], a[4:7], v[108:111], v[4:7]\n"
"ds_read_b32 v231, v220 offset:784\n"
"v_mfma_f32_16x16x32_bf16 v[8:11], a[8:11], v[108:111], v[140:143]\n"
"ds_read_b32 v232, v222 offset:1040\n"
"v_mfma_f32_16x16x32_bf16 v[8:11], a[20:23], v[112:115], v[8:11]\n"
"ds_read_b32 v233, v220 offset:1312\n"
"v_mfma_f32_16x16x32_bf16 v[8:11], a[32:35], v[116:119], v[8:11]\n"
"ds_read_b128 v[108:111], v212 offset:384\n"
"v_mfma_f32_16x16x32_bf16 v[36:39], a[252:255], v[120:123], v[36:39]\n"
"ds_read_b128 v[112:115], v212 offset:448\n"
"v_mfma_f32_16x16x32_bf16 v[36:39], v[152:155], v[124:127], v[36:39]\n"
"ds_read_b128 v[116:119], v212 offset:512\n"
"v_mfma_f32_16x16x32_bf16 v[36:39], v[164:167], v[128:131], v[36:39]\n"
"ds_read_b32 v234, v223 offset:1568\n"
"v_mfma_f32_16x16x32_bf16 v[40:43], v[168:171], v[128:131], v[40:43]\n"
"ds_read_b32 v235, v220 offset:1840\n"
"v_mfma_f32_16x16x32_bf16 v[40:43], v[156:159], v[124:127], v[40:43]\n"
"ds_read_b32 v236, v220 offset:2096\n"
"v_mfma_f32_16x16x32_bf16 v[40:43], v[144:147], v[120:123], v[40:43]\n"
"ds_read_b32 v237, v220 offset:2368\n"
"s_add_u32 m0, s44, 80384\n"
"v_mfma_f32_16x16x32_bf16 v[44:47], v[148:151], v[120:123], v[44:47]\n"
"ds_read_b32 v238, v220 offset:2624\n"
"s_add_u32 s36, s36, s37\n"
"v_mfma_f32_16x16x32_bf16 v[44:47], v[160:163], v[124:127], v[44:47]\n"
"buffer_load_dwordx4 v214, s[4:7], s36 offen offset:0 lds\n"
"v_mfma_f32_16x16x32_bf16 v[44:47], v[172:175], v[128:131], v[44:47]\n"
"ds_read_b32 v239, v221 offset:2880\n"
"v_mfma_f32_16x16x32_bf16 v[48:51], a[240:243], v[128:131], v[48:51]\n"
"ds_read_b32 v240, v220 offset:3152\n"
"v_mfma_f32_16x16x32_bf16 v[48:51], a[228:231], v[124:127], v[48:51]\n"
"ds_read_b32 v241, v222 offset:3408\n"
"v_mfma_f32_16x16x32_bf16 v[48:51], a[216:219], v[120:123], v[48:51]\n"
"ds_read_b32 v242, v220 offset:3680\n"
"v_mfma_f32_16x16x32_bf16 v[52:55], a[220:223], v[120:123], v[52:55]\n"
"ds_read_b32 v243, v223 offset:3936\n"
"v_mfma_f32_16x16x32_bf16 v[52:55], a[232:235], v[124:127], v[52:55]\n"
"ds_read_b32 v244, v220 offset:4208\n"
"v_mfma_f32_16x16x32_bf16 v[52:55], a[244:247], v[128:131], v[52:55]\n"
"ds_read_b32 v245, v220 offset:4464\n"
"v_mfma_f32_16x16x32_bf16 v[56:59], a[248:251], v[128:131], v[56:59]\n"
"v_mfma_f32_16x16x32_bf16 v[56:59], a[236:239], v[124:127], v[56:59]\n"
"v_mfma_f32_16x16x32_bf16 v[56:59], a[224:227], v[120:123], v[56:59]\n"
"v_mfma_f32_16x16x32_bf16 v[72:75], a[144:147], v[120:123], v[72:75]\n"
"v_mfma_f32_16x16x32_bf16 v[72:75], a[156:159], v[124:127], v[72:75]\n"
"s_waitcnt lgkmcnt(0)\n"
"v_mfma_f32_16x16x32_bf16 v[72:75], a[168:171], v[128:131], v[72:75]\n"
"buffer_store_dword v228, v225, s[24:27], s38 offen offset:0 sc1\n"
"v_mfma_f32_16x16x32_bf16 v[76:79], a[172:175], v[128:131], v[76:79]\n"
"v_mfma_f32_16x16x32_bf16 v[76:79], a[160:163], v[124:127], v[76:79]\n"
"v_mfma_f32_16x16x32_bf16 v[76:79], a[148:151], v[120:123], v[76:79]\n"
"v_mfma_f32_16x16x32_bf16 v[80:83], a[152:155], v[120:123], v[80:83]\n"
"v_mfma_f32_16x16x32_bf16 v[80:83], a[164:167], v[124:127], v[80:83]\n"
"v_mfma_f32_16x16x32_bf16 v[80:83], a[176:179], v[128:131], v[80:83]\n"
"v_mfma_f32_16x16x32_bf16 v[84:87], a[132:135], v[128:131], v[84:87]\n"
"v_mfma_f32_16x16x32_bf16 v[84:87], a[120:123], v[124:127], v[84:87]\n"
"v_mfma_f32_16x16x32_bf16 v[84:87], a[108:111], v[120:123], v[84:87]\n"
"v_mfma_f32_16x16x32_bf16 v[88:91], a[112:115], v[120:123], v[88:91]\n"
"v_mfma_f32_16x16x32_bf16 v[88:91], a[124:127], v[124:127], v[88:91]\n"
"buffer_store_dword v229, v225, s[24:27], s38 offen offset:256 sc1\n"
"v_mfma_f32_16x16x32_bf16 v[88:91], a[136:139], v[128:131], v[88:91]\n"
"v_mfma_f32_16x16x32_bf16 v[92:95], a[140:143], v[128:131], v[92:95]\n"
"v_mfma_f32_16x16x32_bf16 v[92:95], a[128:131], v[124:127], v[92:95]\n"
"v_mfma_f32_16x16x32_bf16 v[92:95], a[116:119], v[120:123], v[92:95]\n"
"buffer_load_dwordx4 v215, s[4:7], s36 offen offset:1024 lds\n"
"v_mfma_f32_16x16x32_bf16 v[0:3], a[36:39], v[120:123], v[0:3]\n"
"v_mfma_f32_16x16x32_bf16 v[0:3], a[48:51], v[124:127], v[0:3]\n"
"v_mfma_f32_16x16x32_bf16 v[0:3], a[60:63], v[128:131], v[0:3]\n"
"v_mfma_f32_16x16x32_bf16 v[4:7], a[64:67], v[128:131], v[4:7]\n"
"v_mfma_f32_16x16x32_bf16 v[4:7], a[52:55], v[124:127], v[4:7]\n"
"v_mfma_f32_16x16x32_bf16 v[4:7], a[40:43], v[120:123], v[4:7]\n"
"v_mfma_f32_16x16x32_bf16 v[8:11], a[44:47], v[120:123], v[8:11]\n"
"buffer_store_dword v230, v225, s[24:27], s38 offen offset:512 sc1\n"
"v_mfma_f32_16x16x32_bf16 v[8:11], a[56:59], v[124:127], v[8:11]\n"
"v_mfma_f32_16x16x32_bf16 v[8:11], a[68:71], v[128:131], v[8:11]\n"
"v_mfma_f32_16x16x32_bf16 v[12:15], a[24:27], v[128:131], v[132:135]\n"
"v_mfma_f32_16x16x32_bf16 v[12:15], a[12:15], v[124:127], v[12:15]\n"
"v_mfma_f32_16x16x32_bf16 v[12:15], a[0:3], v[120:123], v[12:15]\n"
"v_mfma_f32_16x16x32_bf16 v[16:19], a[4:7], v[120:123], v[136:139]\n"
"v_mfma_f32_16x16x32_bf16 v[16:19], a[16:19], v[124:127], v[16:19]\n"
"v_mfma_f32_16x16x32_bf16 v[16:19], a[28:31], v[128:131], v[16:19]\n"
"v_mfma_f32_16x16x32_bf16 v[20:23], a[32:35], v[128:131], v[140:143]\n"
"v_mfma_f32_16x16x32_bf16 v[20:23], a[20:23], v[124:127], v[20:23]\n"
"v_mfma_f32_16x16x32_bf16 v[20:23], a[8:11], v[120:123], v[20:23]\n"
"ds_read_b128 v[120:123], v213 offset:576\n"
"v_mfma_f32_16x16x32_bf16 v[36:39], v[176:179], v[108:111], v[36:39]\n"
"buffer_store_dword v231, v225, s[24:27], s38 offen offset:768 sc1\n"
"v_mfma_f32_16x16x32_bf16 v[36:39], v[188:191], v[112:115], v[36:39]\n"
"ds_read_b128 v[124:127], v213 offset:640\n"
"v_mfma_f32_16x16x32_bf16 v[36:39], v[200:203], v[116:119], v[36:39]\n"
"ds_read_b128 v[128:131], v213 offset:704\n"
"v_mfma_f32_16x16x32_bf16 v[40:43], v[204:207], v[116:119], v[40:43]\n"
"v_mfma_f32_16x16x32_bf16 v[40:43], v[192:195], v[112:115], v[40:43]\n"
"v_mfma_f32_16x16x32_bf16 v[40:43], v[180:183], v[108:111], v[40:43]\n"
"v_mfma_f32_16x16x32_bf16 v[44:47], v[184:187], v[108:111], v[44:47]\n"
"v_mfma_f32_16x16x32_bf16 v[44:47], v[196:199], v[112:115], v[44:47]\n"
"v_mfma_f32_16x16x32_bf16 v[44:47], v[208:211], v[116:119], v[44:47]\n"
"v_mfma_f32_16x16x32_bf16 v[48:51], v[164:167], v[116:119], v[48:51]\n"
"buffer_load_dwordx4 v216, s[4:7], s36 offen offset:2048 lds\n"
"v_mfma_f32_16x16x32_bf16 v[48:51], v[152:155], v[112:115], v[48:51]\n"
"v_cvt_pk_bf16_f32 v36, v36, v37\n"
"v_cvt_pk_bf16_f32 v37, v38, v39\n"
"v_mfma_f32_16x16x32_bf16 v[48:51], a[252:255], v[108:111], v[48:51]\n"
"buffer_store_dword v232, v225, s[24:27], s38 offen offset:1024 sc1\n"
"v_mfma_f32_16x16x32_bf16 v[52:55], v[144:147], v[108:111], v[52:55]\n"
"ds_write_b32 v219, v36 offset:59904\n"
"v_mfma_f32_16x16x32_bf16 v[52:55], v[156:159], v[112:115], v[52:55]\n"
"v_mfma_f32_16x16x32_bf16 v[52:55], v[168:171], v[116:119], v[52:55]\n"
"ds_write_b32 v219, v37 offset:59908\n"
"v_mfma_f32_16x16x32_bf16 v[56:59], v[172:175], v[116:119], v[56:59]\n"
"v_cvt_pk_bf16_f32 v40, v40, v41\n"
"v_cvt_pk_bf16_f32 v41, v42, v43\n"
"v_mfma_f32_16x16x32_bf16 v[56:59], v[160:163], v[112:115], v[56:59]\n"
"ds_write_b32 v219, v40 offset:59936\n"
"v_mfma_f32_16x16x32_bf16 v[56:59], v[148:151], v[108:111], v[56:59]\n"
"v_mfma_f32_16x16x32_bf16 v[60:63], a[216:219], v[108:111], v[60:63]\n"
"ds_write_b32 v219, v41 offset:59940\n"
"v_mfma_f32_16x16x32_bf16 v[60:63], a[228:231], v[112:115], v[60:63]\n"
"v_cvt_pk_bf16_f32 v44, v44, v45\n"
"v_cvt_pk_bf16_f32 v45, v46, v47\n"
"v_mfma_f32_16x16x32_bf16 v[60:63], a[240:243], v[116:119], v[60:63]\n"
"ds_write_b32 v219, v44 offset:59968\n"
"v_mfma_f32_16x16x32_bf16 v[64:67], a[244:247], v[116:119], v[64:67]\n"
"v_mfma_f32_16x16x32_bf16 v[64:67], a[232:235], v[112:115], v[64:67]\n"
"buffer_store_dword v233, v225, s[24:27], s38 offen offset:1280 sc1\n"
"v_mfma_f32_16x16x32_bf16 v[64:67], a[220:223], v[108:111], v[64:67]\n"
"ds_write_b32 v219, v45 offset:59972\n"
"v_mfma_f32_16x16x32_bf16 v[68:71], a[224:227], v[108:111], v[68:71]\n"
"v_mfma_f32_16x16x32_bf16 v[68:71], a[236:239], v[112:115], v[68:71]\n"
"v_mfma_f32_16x16x32_bf16 v[68:71], a[248:251], v[116:119], v[68:71]\n"
"v_mfma_f32_16x16x32_bf16 v[72:75], a[204:207], v[116:119], v[72:75]\n"
"v_mfma_f32_16x16x32_bf16 v[72:75], a[192:195], v[112:115], v[72:75]\n"
"v_mfma_f32_16x16x32_bf16 v[72:75], a[180:183], v[108:111], v[72:75]\n"
"v_mfma_f32_16x16x32_bf16 v[76:79], a[184:187], v[108:111], v[76:79]\n"
"v_mfma_f32_16x16x32_bf16 v[76:79], a[196:199], v[112:115], v[76:79]\n"
"v_mfma_f32_16x16x32_bf16 v[76:79], a[208:211], v[116:119], v[76:79]\n"
"v_mfma_f32_16x16x32_bf16 v[80:83], a[212:215], v[116:119], v[80:83]\n"
"buffer_store_dword v234, v225, s[24:27], s38 offen offset:1536 sc1\n"
"v_mfma_f32_16x16x32_bf16 v[80:83], a[200:203], v[112:115], v[80:83]\n"
"v_mfma_f32_16x16x32_bf16 v[80:83], a[188:191], v[108:111], v[80:83]\n"
"v_mfma_f32_16x16x32_bf16 v[84:87], a[144:147], v[108:111], v[84:87]\n"
"v_mfma_f32_16x16x32_bf16 v[84:87], a[156:159], v[112:115], v[84:87]\n"
"buffer_load_dwordx4 v217, s[4:7], s36 offen offset:3072 lds\n"
"v_mfma_f32_16x16x32_bf16 v[84:87], a[168:171], v[116:119], v[84:87]\n"
"v_mfma_f32_16x16x32_bf16 v[88:91], a[172:175], v[116:119], v[88:91]\n"
"v_mfma_f32_16x16x32_bf16 v[88:91], a[160:163], v[112:115], v[88:91]\n"
"v_mfma_f32_16x16x32_bf16 v[88:91], a[148:151], v[108:111], v[88:91]\n"
"v_mfma_f32_16x16x32_bf16 v[92:95], a[152:155], v[108:111], v[92:95]\n"
"v_mfma_f32_16x16x32_bf16 v[92:95], a[164:167], v[112:115], v[92:95]\n"
"v_mfma_f32_16x16x32_bf16 v[92:95], a[176:179], v[116:119], v[92:95]\n"
"buffer_store_dword v235, v225, s[24:27], s38 offen offset:1792 sc1\n"
"v_mfma_f32_16x16x32_bf16 v[96:99], a[132:135], v[116:119], v[96:99]\n"
"v_mfma_f32_16x16x32_bf16 v[96:99], a[120:123], v[112:115], v[96:99]\n"
"v_mfma_f32_16x16x32_bf16 v[96:99], a[108:111], v[108:111], v[96:99]\n"
"v_mfma_f32_16x16x32_bf16 v[100:103], a[112:115], v[108:111], v[100:103]\n"
"v_mfma_f32_16x16x32_bf16 v[100:103], a[124:127], v[112:115], v[100:103]\n"
"v_mfma_f32_16x16x32_bf16 v[100:103], a[136:139], v[116:119], v[100:103]\n"
"v_mfma_f32_16x16x32_bf16 v[104:107], a[140:143], v[116:119], v[104:107]\n"
"v_mfma_f32_16x16x32_bf16 v[104:107], a[128:131], v[112:115], v[104:107]\n"
"v_mfma_f32_16x16x32_bf16 v[104:107], a[116:119], v[108:111], v[104:107]\n"
"v_mfma_f32_16x16x32_bf16 v[0:3], a[72:75], v[108:111], v[0:3]\n"
"v_mfma_f32_16x16x32_bf16 v[0:3], a[84:87], v[112:115], v[0:3]\n"
"buffer_store_dword v236, v225, s[24:27], s38 offen offset:2048 sc1\n"
"v_mfma_f32_16x16x32_bf16 v[0:3], a[96:99], v[116:119], v[0:3]\n"
"v_mfma_f32_16x16x32_bf16 v[4:7], a[100:103], v[116:119], v[4:7]\n"
"v_mfma_f32_16x16x32_bf16 v[4:7], a[88:91], v[112:115], v[4:7]\n"
"v_mfma_f32_16x16x32_bf16 v[4:7], a[76:79], v[108:111], v[4:7]\n"
"v_mfma_f32_16x16x32_bf16 v[8:11], a[80:83], v[108:111], v[8:11]\n"
"v_mfma_f32_16x16x32_bf16 v[8:11], a[92:95], v[112:115], v[8:11]\n"
"v_mfma_f32_16x16x32_bf16 v[8:11], a[104:107], v[116:119], v[8:11]\n"
"s_add_u32 m0, s45, 96768\n"
"v_mfma_f32_16x16x32_bf16 v[12:15], a[60:63], v[116:119], v[12:15]\n"
"v_mfma_f32_16x16x32_bf16 v[12:15], a[48:51], v[112:115], v[12:15]\n"
"v_mfma_f32_16x16x32_bf16 v[12:15], a[36:39], v[108:111], v[12:15]\n"
"buffer_load_dwordx4 v218, s[4:7], s36 offen lds\n"
"v_mfma_f32_16x16x32_bf16 v[16:19], a[40:43], v[108:111], v[16:19]\n"
"buffer_store_dword v237, v225, s[24:27], s38 offen offset:2304 sc1\n"
"v_mfma_f32_16x16x32_bf16 v[16:19], a[52:55], v[112:115], v[16:19]\n"
"v_mfma_f32_16x16x32_bf16 v[16:19], a[64:67], v[116:119], v[16:19]\n"
"v_mfma_f32_16x16x32_bf16 v[20:23], a[68:71], v[116:119], v[20:23]\n"
"v_mfma_f32_16x16x32_bf16 v[20:23], a[56:59], v[112:115], v[20:23]\n"
"v_mfma_f32_16x16x32_bf16 v[20:23], a[44:47], v[108:111], v[20:23]\n"
"v_mfma_f32_16x16x32_bf16 v[24:27], a[0:3], v[108:111], v[132:135]\n"
"v_mfma_f32_16x16x32_bf16 v[24:27], a[12:15], v[112:115], v[24:27]\n"
"v_mfma_f32_16x16x32_bf16 v[24:27], a[24:27], v[116:119], v[24:27]\n"
"v_mfma_f32_16x16x32_bf16 v[28:31], a[28:31], v[116:119], v[136:139]\n"
"v_mfma_f32_16x16x32_bf16 v[28:31], a[16:19], v[112:115], v[28:31]\n"
"v_mfma_f32_16x16x32_bf16 v[28:31], a[4:7], v[108:111], v[28:31]\n"
"buffer_store_dword v238, v225, s[24:27], s38 offen offset:2560 sc1\n"
"v_mfma_f32_16x16x32_bf16 v[32:35], a[8:11], v[108:111], v[140:143]\n"
"v_mfma_f32_16x16x32_bf16 v[32:35], a[20:23], v[112:115], v[32:35]\n"
"v_mfma_f32_16x16x32_bf16 v[32:35], a[32:35], v[116:119], v[32:35]\n"
"ds_read_b128 v[108:111], v213 offset:768\n"
"s_waitcnt lgkmcnt(7)\n"
"v_mfma_f32_16x16x32_bf16 v[48:51], v[176:179], v[120:123], v[48:51]\n"
"ds_read_b128 v[112:115], v213 offset:832\n"
"v_mfma_f32_16x16x32_bf16 v[48:51], v[188:191], v[124:127], v[48:51]\n"
"ds_read_b128 v[116:119], v213 offset:896\n"
"v_mfma_f32_16x16x32_bf16 v[48:51], v[200:203], v[128:131], v[48:51]\n"
"v_mfma_f32_16x16x32_bf16 v[52:55], v[204:207], v[128:131], v[52:55]\n"
"v_mfma_f32_16x16x32_bf16 v[52:55], v[192:195], v[124:127], v[52:55]\n"
"v_mfma_f32_16x16x32_bf16 v[52:55], v[180:183], v[120:123], v[52:55]\n"
"v_mfma_f32_16x16x32_bf16 v[56:59], v[184:187], v[120:123], v[56:59]\n"
"v_mfma_f32_16x16x32_bf16 v[56:59], v[196:199], v[124:127], v[56:59]\n"
"buffer_store_dword v239, v225, s[24:27], s38 offen offset:2816 sc1\n"
"v_mfma_f32_16x16x32_bf16 v[56:59], v[208:211], v[128:131], v[56:59]\n"
"v_mfma_f32_16x16x32_bf16 v[60:63], v[164:167], v[128:131], v[60:63]\n"
"v_mfma_f32_16x16x32_bf16 v[60:63], v[152:155], v[124:127], v[60:63]\n"
"v_cvt_pk_bf16_f32 v48, v48, v49\n"
"v_cvt_pk_bf16_f32 v49, v50, v51\n"
"v_mfma_f32_16x16x32_bf16 v[60:63], a[252:255], v[120:123], v[60:63]\n"
"ds_write_b32 v219, v48 offset:60096\n"
"v_mfma_f32_16x16x32_bf16 v[64:67], v[144:147], v[120:123], v[64:67]\n"
"v_mfma_f32_16x16x32_bf16 v[64:67], v[156:159], v[124:127], v[64:67]\n"
"ds_write_b32 v219, v49 offset:60100\n"
"v_mfma_f32_16x16x32_bf16 v[64:67], v[168:171], v[128:131], v[64:67]\n"
"v_cvt_pk_bf16_f32 v52, v52, v53\n"
"v_cvt_pk_bf16_f32 v53, v54, v55\n"
"v_mfma_f32_16x16x32_bf16 v[68:71], v[172:175], v[128:131], v[68:71]\n"
"ds_write_b32 v219, v52 offset:60128\n"
"v_mfma_f32_16x16x32_bf16 v[68:71], v[160:163], v[124:127], v[68:71]\n"
"v_mfma_f32_16x16x32_bf16 v[68:71], v[148:151], v[120:123], v[68:71]\n"
"ds_write_b32 v219, v53 offset:60132\n"
"v_mfma_f32_16x16x32_bf16 v[84:87], a[180:183], v[120:123], v[84:87]\n"
"buffer_store_dword v240, v225, s[24:27], s38 offen offset:3072 sc1\n"
"v_mfma_f32_16x16x32_bf16 v[84:87], a[192:195], v[124:127], v[84:87]\n"
"v_cvt_pk_bf16_f32 v56, v56, v57\n"
"v_cvt_pk_bf16_f32 v57, v58, v59\n"
"v_mfma_f32_16x16x32_bf16 v[84:87], a[204:207], v[128:131], v[84:87]\n"
"ds_write_b32 v219, v56 offset:60160\n"
"v_mfma_f32_16x16x32_bf16 v[88:91], a[208:211], v[128:131], v[88:91]\n"
"s_add_u32 m0, s44, 100864\n"
"s_add_u32 s36, s36, s37\n"
"v_mfma_f32_16x16x32_bf16 v[88:91], a[196:199], v[124:127], v[88:91]\n"
"buffer_load_dwordx4 v214, s[16:19], s36 offen offset:0 lds\n"
"v_mfma_f32_16x16x32_bf16 v[88:91], a[184:187], v[120:123], v[88:91]\n"
"ds_write_b32 v219, v57 offset:60164\n"
"v_mfma_f32_16x16x32_bf16 v[92:95], a[188:191], v[120:123], v[92:95]\n"
"v_mfma_f32_16x16x32_bf16 v[92:95], a[200:203], v[124:127], v[92:95]\n"
"v_mfma_f32_16x16x32_bf16 v[92:95], a[212:215], v[128:131], v[92:95]\n"
"v_mfma_f32_16x16x32_bf16 v[96:99], a[168:171], v[128:131], v[96:99]\n"
"v_mfma_f32_16x16x32_bf16 v[96:99], a[156:159], v[124:127], v[96:99]\n"
"v_mfma_f32_16x16x32_bf16 v[96:99], a[144:147], v[120:123], v[96:99]\n"
"buffer_store_dword v241, v225, s[24:27], s38 offen offset:3328 sc1\n"
"v_mfma_f32_16x16x32_bf16 v[100:103], a[148:151], v[120:123], v[100:103]\n"
"v_mfma_f32_16x16x32_bf16 v[100:103], a[160:163], v[124:127], v[100:103]\n"
"v_mfma_f32_16x16x32_bf16 v[100:103], a[172:175], v[128:131], v[100:103]\n"
"v_mfma_f32_16x16x32_bf16 v[104:107], a[176:179], v[128:131], v[104:107]\n"
"v_mfma_f32_16x16x32_bf16 v[104:107], a[164:167], v[124:127], v[104:107]\n"
"v_mfma_f32_16x16x32_bf16 v[104:107], a[152:155], v[120:123], v[104:107]\n"
"v_mfma_f32_16x16x32_bf16 v[12:15], a[72:75], v[120:123], v[12:15]\n"
"v_mfma_f32_16x16x32_bf16 v[12:15], a[84:87], v[124:127], v[12:15]\n"
"v_mfma_f32_16x16x32_bf16 v[12:15], a[96:99], v[128:131], v[12:15]\n"
"v_mfma_f32_16x16x32_bf16 v[16:19], a[100:103], v[128:131], v[16:19]\n"
"v_mfma_f32_16x16x32_bf16 v[16:19], a[88:91], v[124:127], v[16:19]\n"
"buffer_store_dword v242, v225, s[24:27], s38 offen offset:3584 sc1\n"
"v_mfma_f32_16x16x32_bf16 v[16:19], a[76:79], v[120:123], v[16:19]\n"
"v_mfma_f32_16x16x32_bf16 v[20:23], a[80:83], v[120:123], v[20:23]\n"
"v_mfma_f32_16x16x32_bf16 v[20:23], a[92:95], v[124:127], v[20:23]\n"
"v_mfma_f32_16x16x32_bf16 v[20:23], a[104:107], v[128:131], v[20:23]\n"
"v_mfma_f32_16x16x32_bf16 v[24:27], a[60:63], v[128:131], v[24:27]\n"
"v_mfma_f32_16x16x32_bf16 v[24:27], a[48:51], v[124:127], v[24:27]\n"
"v_mfma_f32_16x16x32_bf16 v[24:27], a[36:39], v[120:123], v[24:27]\n"
"v_mfma_f32_16x16x32_bf16 v[28:31], a[40:43], v[120:123], v[28:31]\n"
"v_mfma_f32_16x16x32_bf16 v[28:31], a[52:55], v[124:127], v[28:31]\n"
"v_mfma_f32_16x16x32_bf16 v[28:31], a[64:67], v[128:131], v[28:31]\n"
"buffer_load_dwordx4 v215, s[16:19], s36 offen offset:1024 lds\n"
"v_mfma_f32_16x16x32_bf16 v[32:35], a[68:71], v[128:131], v[32:35]\n"
"buffer_store_dword v243, v225, s[24:27], s38 offen offset:3840 sc1\n"
"v_mfma_f32_16x16x32_bf16 v[32:35], a[56:59], v[124:127], v[32:35]\n"
"v_mfma_f32_16x16x32_bf16 v[32:35], a[44:47], v[120:123], v[32:35]\n"
"v_add_u32 v212, 143360, v212\n"
"s_waitcnt lgkmcnt(6)\n"
"v_mfma_f32_16x16x32_bf16 v[60:63], v[176:179], v[108:111], v[60:63]\n"
"ds_read_b128 v[120:123], v212 offset:0\n"
"v_mfma_f32_16x16x32_bf16 v[60:63], v[188:191], v[112:115], v[60:63]\n"
"ds_read_b128 v[124:127], v212 offset:64\n"
"v_mfma_f32_16x16x32_bf16 v[60:63], v[200:203], v[116:119], v[60:63]\n"
"ds_read_b128 v[128:131], v212 offset:128\n"
"v_mfma_f32_16x16x32_bf16 v[64:67], v[204:207], v[116:119], v[64:67]\n"
"v_mfma_f32_16x16x32_bf16 v[64:67], v[192:195], v[112:115], v[64:67]\n"
"v_mfma_f32_16x16x32_bf16 v[64:67], v[180:183], v[108:111], v[64:67]\n"
"v_mfma_f32_16x16x32_bf16 v[68:71], v[184:187], v[108:111], v[68:71]\n"
"v_mfma_f32_16x16x32_bf16 v[68:71], v[196:199], v[112:115], v[68:71]\n"
"v_mfma_f32_16x16x32_bf16 v[68:71], v[208:211], v[116:119], v[68:71]\n"
"buffer_store_dword v244, v226, s[24:27], s38 offen offset:0 sc1\n"
"v_mfma_f32_16x16x32_bf16 v[96:99], a[204:207], v[116:119], v[96:99]\n"
"v_mfma_f32_16x16x32_bf16 v[96:99], a[192:195], v[112:115], v[96:99]\n"
"v_cvt_pk_bf16_f32 v60, v60, v61\n"
"v_cvt_pk_bf16_f32 v61, v62, v63\n"
"v_mfma_f32_16x16x32_bf16 v[96:99], a[180:183], v[108:111], v[96:99]\n"
"ds_write_b32 v219, v60 offset:60288\n"
"v_mfma_f32_16x16x32_bf16 v[100:103], a[184:187], v[108:111], v[100:103]\n"
"v_mfma_f32_16x16x32_bf16 v[100:103], a[196:199], v[112:115], v[100:103]\n"
"ds_write_b32 v219, v61 offset:60292\n"
"v_mfma_f32_16x16x32_bf16 v[100:103], a[208:211], v[116:119], v[100:103]\n"
"v_cvt_pk_bf16_f32 v64, v64, v65\n"
"v_cvt_pk_bf16_f32 v65, v66, v67\n"
"v_mfma_f32_16x16x32_bf16 v[104:107], a[212:215], v[116:119], v[104:107]\n"
"ds_write_b32 v219, v64 offset:60320\n"
"v_mfma_f32_16x16x32_bf16 v[104:107], a[200:203], v[112:115], v[104:107]\n"
"v_mfma_f32_16x16x32_bf16 v[104:107], a[188:191], v[108:111], v[104:107]\n"
"ds_write_b32 v219, v65 offset:60324\n"
"v_mfma_f32_16x16x32_bf16 v[24:27], a[72:75], v[108:111], v[24:27]\n"
"v_cvt_pk_bf16_f32 v68, v68, v69\n"
"v_cvt_pk_bf16_f32 v69, v70, v71\n"
"v_mfma_f32_16x16x32_bf16 v[24:27], a[84:87], v[112:115], v[24:27]\n"
"buffer_store_dword v245, v226, s[24:27], s38 offen offset:256 sc1\n"
"v_mfma_f32_16x16x32_bf16 v[24:27], a[96:99], v[116:119], v[24:27]\n"
"ds_write_b32 v219, v68 offset:60352\n"
"v_mfma_f32_16x16x32_bf16 v[28:31], a[100:103], v[116:119], v[28:31]\n"
"v_mfma_f32_16x16x32_bf16 v[28:31], a[88:91], v[112:115], v[28:31]\n"
"ds_write_b32 v219, v69 offset:60356\n"
"v_mfma_f32_16x16x32_bf16 v[28:31], a[76:79], v[108:111], v[28:31]\n"
"v_mfma_f32_16x16x32_bf16 v[32:35], a[80:83], v[108:111], v[32:35]\n"
"buffer_load_dwordx4 v216, s[16:19], s36 offen offset:2048 lds\n"
"v_mfma_f32_16x16x32_bf16 v[32:35], a[92:95], v[112:115], v[32:35]\n"
"v_mfma_f32_16x16x32_bf16 v[32:35], a[104:107], v[116:119], v[32:35]\n"
"ds_read_b128 v[108:111], v212 offset:192\n"
"s_waitcnt lgkmcnt(7)\n"
"v_mfma_f32_16x16x32_bf16 v[72:75], a[216:219], v[120:123], v[72:75]\n"
"ds_read_b128 v[112:115], v212 offset:256\n"
"v_mfma_f32_16x16x32_bf16 v[72:75], a[228:231], v[124:127], v[72:75]\n"
"s_add_u32 s38, s38, s37\n"
"s_waitcnt vmcnt(26) lgkmcnt(2)\n"
"s_barrier\n"
"v_mfma_f32_16x16x32_bf16 v[72:75], a[240:243], v[128:131], v[72:75]\n"
"ds_read_b128 v[116:119], v212 offset:320\n"
"v_mfma_f32_16x16x32_bf16 v[76:79], a[244:247], v[128:131], v[76:79]\n"
"ds_read_b32 v228, v220 offset:59904\n"
"v_mfma_f32_16x16x32_bf16 v[76:79], a[232:235], v[124:127], v[76:79]\n"
"ds_read_b32 v229, v220 offset:60160\n"
"v_mfma_f32_16x16x32_bf16 v[76:79], a[220:223], v[120:123], v[76:79]\n"
"ds_read_b32 v230, v221 offset:60416\n"
"v_mfma_f32_16x16x32_bf16 v[80:83], a[224:227], v[120:123], v[80:83]\n"
"ds_read_b32 v231, v220 offset:60688\n"
"v_mfma_f32_16x16x32_bf16 v[80:83], a[236:239], v[124:127], v[80:83]\n"
"ds_read_b32 v232, v222 offset:60944\n"
"v_mfma_f32_16x16x32_bf16 v[80:83], a[248:251], v[128:131], v[80:83]\n"
"ds_read_b32 v233, v220 offset:61216\n"
"v_mfma_f32_16x16x32_bf16 v[0:3], a[132:135], v[128:131], v[0:3]\n"
"ds_read_b32 v234, v223 offset:61472\n"
"v_mfma_f32_16x16x32_bf16 v[0:3], a[120:123], v[124:127], v[0:3]\n"
"ds_read_b32 v235, v220 offset:61744\n"
"v_mfma_f32_16x16x32_bf16 v[0:3], a[108:111], v[120:123], v[0:3]\n"
"ds_read_b32 v236, v220 offset:62000\n"
"v_mfma_f32_16x16x32_bf16 v[4:7], a[112:115], v[120:123], v[4:7]\n"
"ds_read_b32 v237, v220 offset:62272\n"
"v_mfma_f32_16x16x32_bf16 v[4:7], a[124:127], v[124:127], v[4:7]\n"
"ds_read_b32 v238, v220 offset:62528\n"
"v_mfma_f32_16x16x32_bf16 v[4:7], a[136:139], v[128:131], v[4:7]\n"
"ds_read_b32 v239, v221 offset:62784\n"
"v_mfma_f32_16x16x32_bf16 v[8:11], a[140:143], v[128:131], v[8:11]\n"
"ds_read_b32 v240, v220 offset:63056\n"
"v_mfma_f32_16x16x32_bf16 v[8:11], a[128:131], v[124:127], v[8:11]\n"
"ds_read_b32 v241, v222 offset:63312\n"
"v_mfma_f32_16x16x32_bf16 v[8:11], a[116:119], v[120:123], v[8:11]\n"
"ds_read_b32 v242, v220 offset:63584\n"
"v_mfma_f32_16x16x32_bf16 v[36:39], a[0:3], v[120:123], v[132:135]\n"
"ds_read_b32 v243, v223 offset:63840\n"
"v_mfma_f32_16x16x32_bf16 v[36:39], a[12:15], v[124:127], v[36:39]\n"
"ds_read_b32 v244, v220 offset:64112\n"
"v_mfma_f32_16x16x32_bf16 v[36:39], a[24:27], v[128:131], v[36:39]\n"
"ds_read_b32 v245, v220 offset:64368\n"
"v_mfma_f32_16x16x32_bf16 v[40:43], a[28:31], v[128:131], v[136:139]\n"
"v_mfma_f32_16x16x32_bf16 v[40:43], a[16:19], v[124:127], v[40:43]\n"
"v_mfma_f32_16x16x32_bf16 v[40:43], a[4:7], v[120:123], v[40:43]\n"
"v_mfma_f32_16x16x32_bf16 v[44:47], a[8:11], v[120:123], v[140:143]\n"
"v_mfma_f32_16x16x32_bf16 v[44:47], a[20:23], v[124:127], v[44:47]\n"
"buffer_load_dwordx4 v217, s[16:19], s36 offen offset:3072 lds\n"
"v_mfma_f32_16x16x32_bf16 v[44:47], a[32:35], v[128:131], v[44:47]\n"
"ds_read_b128 v[120:123], v212 offset:384\n"
"s_waitcnt lgkmcnt(15)\n"
"v_mfma_f32_16x16x32_bf16 v[72:75], a[252:255], v[108:111], v[72:75]\n"
"ds_read_b128 v[124:127], v212 offset:448\n"
"s_waitcnt lgkmcnt(2)\n"
"v_mfma_f32_16x16x32_bf16 v[72:75], v[152:155], v[112:115], v[72:75]\n"
"buffer_store_dword v228, v225, s[24:27], s38 offen offset:0 sc1\n"
"v_mfma_f32_16x16x32_bf16 v[72:75], v[164:167], v[116:119], v[72:75]\n"
"ds_read_b128 v[128:131], v212 offset:512\n"
"v_mfma_f32_16x16x32_bf16 v[76:79], v[168:171], v[116:119], v[76:79]\n"
"v_mfma_f32_16x16x32_bf16 v[76:79], v[156:159], v[112:115], v[76:79]\n"
"v_mfma_f32_16x16x32_bf16 v[76:79], v[144:147], v[108:111], v[76:79]\n"
"v_mfma_f32_16x16x32_bf16 v[80:83], v[148:151], v[108:111], v[80:83]\n"
"v_mfma_f32_16x16x32_bf16 v[80:83], v[160:163], v[112:115], v[80:83]\n"
"v_mfma_f32_16x16x32_bf16 v[80:83], v[172:175], v[116:119], v[80:83]\n"
"v_mfma_f32_16x16x32_bf16 v[84:87], a[240:243], v[116:119], v[84:87]\n"
"v_mfma_f32_16x16x32_bf16 v[84:87], a[228:231], v[112:115], v[84:87]\n"
"v_mfma_f32_16x16x32_bf16 v[84:87], a[216:219], v[108:111], v[84:87]\n"
"v_mfma_f32_16x16x32_bf16 v[88:91], a[220:223], v[108:111], v[88:91]\n"
"buffer_store_dword v229, v225, s[24:27], s38 offen offset:256 sc1\n"
"v_mfma_f32_16x16x32_bf16 v[88:91], a[232:235], v[112:115], v[88:91]\n"
"v_mfma_f32_16x16x32_bf16 v[88:91], a[244:247], v[116:119], v[88:91]\n"
"v_mfma_f32_16x16x32_bf16 v[92:95], a[248:251], v[116:119], v[92:95]\n"
"v_mfma_f32_16x16x32_bf16 v[92:95], a[236:239], v[112:115], v[92:95]\n"
"v_mfma_f32_16x16x32_bf16 v[92:95], a[224:227], v[108:111], v[92:95]\n"
"v_mfma_f32_16x16x32_bf16 v[0:3], a[144:147], v[108:111], v[0:3]\n"
"v_mfma_f32_16x16x32_bf16 v[0:3], a[156:159], v[112:115], v[0:3]\n"
"v_mfma_f32_16x16x32_bf16 v[0:3], a[168:171], v[116:119], v[0:3]\n"
"v_mfma_f32_16x16x32_bf16 v[4:7], a[172:175], v[116:119], v[4:7]\n"
"v_mfma_f32_16x16x32_bf16 v[4:7], a[160:163], v[112:115], v[4:7]\n"
"v_mfma_f32_16x16x32_bf16 v[4:7], a[148:151], v[108:111], v[4:7]\n"
"buffer_store_dword v230, v225, s[24:27], s38 offen offset:512 sc1\n"
"v_mfma_f32_16x16x32_bf16 v[8:11], a[152:155], v[108:111], v[8:11]\n"
"s_add_u32 m0, s45, 117248\n"
"v_mfma_f32_16x16x32_bf16 v[8:11], a[164:167], v[112:115], v[8:11]\n"
"v_mfma_f32_16x16x32_bf16 v[8:11], a[176:179], v[116:119], v[8:11]\n"
"buffer_load_dwordx4 v218, s[16:19], s36 offen lds\n"
"v_mfma_f32_16x16x32_bf16 v[12:15], a[132:135], v[116:119], v[12:15]\n"
"v_mfma_f32_16x16x32_bf16 v[12:15], a[120:123], v[112:115], v[12:15]\n"
"v_mfma_f32_16x16x32_bf16 v[12:15], a[108:111], v[108:111], v[12:15]\n"
"v_mfma_f32_16x16x32_bf16 v[16:19], a[112:115], v[108:111], v[16:19]\n"
"v_mfma_f32_16x16x32_bf16 v[16:19], a[124:127], v[112:115], v[16:19]\n"
"v_mfma_f32_16x16x32_bf16 v[16:19], a[136:139], v[116:119], v[16:19]\n"
"v_mfma_f32_16x16x32_bf16 v[20:23], a[140:143], v[116:119], v[20:23]\n"
"v_mfma_f32_16x16x32_bf16 v[20:23], a[128:131], v[112:115], v[20:23]\n"
"buffer_store_dword v231, v225, s[24:27], s38 offen offset:768 sc1\n"
"v_mfma_f32_16x16x32_bf16 v[20:23], a[116:119], v[108:111], v[20:23]\n"
"v_mfma_f32_16x16x32_bf16 v[36:39], a[36:39], v[108:111], v[36:39]\n"
"v_mfma_f32_16x16x32_bf16 v[36:39], a[48:51], v[112:115], v[36:39]\n"
"v_mfma_f32_16x16x32_bf16 v[36:39], a[60:63], v[116:119], v[36:39]\n"
"v_mfma_f32_16x16x32_bf16 v[40:43], a[64:67], v[116:119], v[40:43]\n"
"v_mfma_f32_16x16x32_bf16 v[40:43], a[52:55], v[112:115], v[40:43]\n"
"v_mfma_f32_16x16x32_bf16 v[40:43], a[40:43], v[108:111], v[40:43]\n"
"v_mfma_f32_16x16x32_bf16 v[44:47], a[44:47], v[108:111], v[44:47]\n"
"v_mfma_f32_16x16x32_bf16 v[44:47], a[56:59], v[112:115], v[44:47]\n"
"v_mfma_f32_16x16x32_bf16 v[44:47], a[68:71], v[116:119], v[44:47]\n"
"v_mfma_f32_16x16x32_bf16 v[48:51], a[24:27], v[116:119], v[132:135]\n"
"buffer_store_dword v232, v225, s[24:27], s38 offen offset:1024 sc1\n"
"v_mfma_f32_16x16x32_bf16 v[48:51], a[12:15], v[112:115], v[48:51]\n"
"v_mfma_f32_16x16x32_bf16 v[48:51], a[0:3], v[108:111], v[48:51]\n"
"v_mfma_f32_16x16x32_bf16 v[52:55], a[4:7], v[108:111], v[136:139]\n"
"v_mfma_f32_16x16x32_bf16 v[52:55], a[16:19], v[112:115], v[52:55]\n"
"v_mfma_f32_16x16x32_bf16 v[52:55], a[28:31], v[116:119], v[52:55]\n"
"v_mfma_f32_16x16x32_bf16 v[56:59], a[32:35], v[116:119], v[140:143]\n"
"v_mfma_f32_16x16x32_bf16 v[56:59], a[20:23], v[112:115], v[56:59]\n"
"v_mfma_f32_16x16x32_bf16 v[56:59], a[8:11], v[108:111], v[56:59]\n"
"v_add_u32 v213, 143360, v213\n"
"s_waitcnt lgkmcnt(0)\n"
"v_mfma_f32_16x16x32_bf16 v[72:75], v[176:179], v[120:123], v[72:75]\n"
"ds_read_b128 v[108:111], v213 offset:576\n"
"v_mfma_f32_16x16x32_bf16 v[72:75], v[188:191], v[124:127], v[72:75]\n"
"ds_read_b128 v[112:115], v213 offset:640\n"
"v_mfma_f32_16x16x32_bf16 v[72:75], v[200:203], v[128:131], v[72:75]\n"
"buffer_store_dword v233, v225, s[24:27], s38 offen offset:1280 sc1\n"
"v_mfma_f32_16x16x32_bf16 v[76:79], v[204:207], v[128:131], v[76:79]\n"
"ds_read_b128 v[116:119], v213 offset:704\n"
"v_mfma_f32_16x16x32_bf16 v[76:79], v[192:195], v[124:127], v[76:79]\n"
"v_mfma_f32_16x16x32_bf16 v[76:79], v[180:183], v[120:123], v[76:79]\n"
"v_mfma_f32_16x16x32_bf16 v[80:83], v[184:187], v[120:123], v[80:83]\n"
"v_mfma_f32_16x16x32_bf16 v[80:83], v[196:199], v[124:127], v[80:83]\n"
"v_mfma_f32_16x16x32_bf16 v[80:83], v[208:211], v[128:131], v[80:83]\n"
"v_mfma_f32_16x16x32_bf16 v[84:87], v[164:167], v[128:131], v[84:87]\n"
"v_mfma_f32_16x16x32_bf16 v[84:87], v[152:155], v[124:127], v[84:87]\n"
"v_cvt_pk_bf16_f32 v72, v72, v73\n"
"v_cvt_pk_bf16_f32 v73, v74, v75\n"
"v_mfma_f32_16x16x32_bf16 v[84:87], a[252:255], v[120:123], v[84:87]\n"
"ds_write_b32 v219, v72 offset:0\n"
"v_mfma_f32_16x16x32_bf16 v[88:91], v[144:147], v[120:123], v[88:91]\n"
"v_mfma_f32_16x16x32_bf16 v[88:91], v[156:159], v[124:127], v[88:91]\n"
"buffer_store_dword v234, v225, s[24:27], s38 offen offset:1536 sc1\n"
"v_mfma_f32_16x16x32_bf16 v[88:91], v[168:171], v[128:131], v[88:91]\n"
"ds_write_b32 v219, v73 offset:4\n"
"v_mfma_f32_16x16x32_bf16 v[92:95], v[172:175], v[128:131], v[92:95]\n"
"v_cvt_pk_bf16_f32 v76, v76, v77\n"
"v_cvt_pk_bf16_f32 v77, v78, v79\n"
".Lconv_stub2:\n"
"s_cbranch_vccnz .Lconv_ret\n"
"v_mfma_f32_16x16x32_bf16 v[92:95], v[160:163], v[124:127], v[92:95]\n"
"ds_write_b32 v219, v76 offset:32\n"
"v_mfma_f32_16x16x32_bf16 v[92:95], v[148:151], v[120:123], v[92:95]\n"
"v_mfma_f32_16x16x32_bf16 v[96:99], a[216:219], v[120:123], v[96:99]\n"
"ds_write_b32 v219, v77 offset:36\n"
"v_mfma_f32_16x16x32_bf16 v[96:99], a[228:231], v[124:127], v[96:99]\n"
"v_cvt_pk_bf16_f32 v80, v80, v81\n"
"v_cvt_pk_bf16_f32 v81, v82, v83\n"
"v_mfma_f32_16x16x32_bf16 v[96:99], a[240:243], v[128:131], v[96:99]\n"
"ds_write_b32 v219, v80 offset:64\n"
"v_mfma_f32_16x16x32_bf16 v[100:103], a[244:247], v[128:131], v[100:103]\n"
"v_mfma_f32_16x16x32_bf16 v[100:103], a[232:235], v[124:127], v[100:103]\n"
"ds_write_b32 v219, v81 offset:68\n"
"v_mfma_f32_16x16x32_bf16 v[100:103], a[220:223], v[120:123], v[100:103]\n"
"v_mfma_f32_16x16x32_bf16 v[104:107], a[224:227], v[120:123], v[104:107]\n"
"buffer_store_dword v235, v225, s[24:27], s38 offen offset:1792 sc1\n"
"v_mfma_f32_16x16x32_bf16 v[104:107], a[236:239], v[124:127], v[104:107]\n"
"v_mfma_f32_16x16x32_bf16 v[104:107], a[248:251], v[128:131], v[104:107]\n"
"v_mfma_f32_16x16x32_bf16 v[0:3], a[204:207], v[128:131], v[0:3]\n"
"v_mfma_f32_16x16x32_bf16 v[0:3], a[192:195], v[124:127], v[0:3]\n"
"v_mfma_f32_16x16x32_bf16 v[0:3], a[180:183], v[120:123], v[0:3]\n"
"v_mfma_f32_16x16x32_bf16 v[4:7], a[184:187], v[120:123], v[4:7]\n"
"v_mfma_f32_16x16x32_bf16 v[4:7], a[196:199], v[124:127], v[4:7]\n"
"v_mfma_f32_16x16x32_bf16 v[4:7], a[208:211], v[128:131], v[4:7]\n"
"v_mfma_f32_16x16x32_bf16 v[8:11], a[212:215], v[128:131], v[8:11]\n"
"v_mfma_f32_16x16x32_bf16 v[8:11], a[200:203], v[124:127], v[8:11]\n"
"v_mfma_f32_16x16x32_bf16 v[8:11], a[188:191], v[120:123], v[8:11]\n"
"buffer_store_dword v236, v225, s[24:27], s38 offen offset:2048 sc1\n"
"v_mfma_f32_16x16x32_bf16 v[12:15], a[144:147], v[120:123], v[12:15]\n"
"v_mfma_f32_16x16x32_bf16 v[12:15], a[156:159], v[124:127], v[12:15]\n"
"v_mfma_f32_16x16x32_bf16 v[12:15], a[168:171], v[128:131], v[12:15]\n"
"v_mfma_f32_16x16x32_bf16 v[16:19], a[172:175], v[128:131], v[16:19]\n"
"v_mfma_f32_16x16x32_bf16 v[16:19], a[160:163], v[124:127], v[16:19]\n"
"v_mfma_f32_16x16x32_bf16 v[16:19], a[148:151], v[120:123], v[16:19]\n"
"v_mfma_f32_16x16x32_bf16 v[20:23], a[152:155], v[120:123], v[20:23]\n"
"v_mfma_f32_16x16x32_bf16 v[20:23], a[164:167], v[124:127], v[20:23]\n"
"v_mfma_f32_16x16x32_bf16 v[20:23], a[176:179], v[128:131], v[20:23]\n"
"v_mfma_f32_16x16x32_bf16 v[24:27], a[132:135], v[128:131], v[24:27]\n"
"v_mfma_f32_16x16x32_bf16 v[24:27], a[120:123], v[124:127], v[24:27]\n"
"buffer_store_dword v237, v225, s[24:27], s38 offen offset:2304 sc1\n"
"v_mfma_f32_16x16x32_bf16 v[24:27], a[108:111], v[120:123], v[24:27]\n"
"v_mfma_f32_16x16x32_bf16 v[28:31], a[112:115], v[120:123], v[28:31]\n"
"v_mfma_f32_16x16x32_bf16 v[28:31], a[124:127], v[124:127], v[28:31]\n"
"v_mfma_f32_16x16x32_bf16 v[28:31], a[136:139], v[128:131], v[28:31]\n"
"v_mfma_f32_16x16x32_bf16 v[32:35], a[140:143], v[128:131], v[32:35]\n"
"v_mfma_f32_16x16x32_bf16 v[32:35], a[128:131], v[124:127], v[32:35]\n"
"v_mfma_f32_16x16x32_bf16 v[32:35], a[116:119], v[120:123], v[32:35]\n"
"v_mfma_f32_16x16x32_bf16 v[36:39], a[72:75], v[120:123], v[36:39]\n"
"v_mfma_f32_16x16x32_bf16 v[36:39], a[84:87], v[124:127], v[36:39]\n"
"v_mfma_f32_16x16x32_bf16 v[36:39], a[96:99], v[128:131], v[36:39]\n"
"v_mfma_f32_16x16x32_bf16 v[40:43], a[100:103], v[128:131], v[40:43]\n"
"buffer_store_dword v238, v225, s[24:27], s38 offen offset:2560 sc1\n"
"v_mfma_f32_16x16x32_bf16 v[40:43], a[88:91], v[124:127], v[40:43]\n"
"v_mfma_f32_16x16x32_bf16 v[40:43], a[76:79], v[120:123], v[40:43]\n"
"v_mfma_f32_16x16x32_bf16 v[44:47], a[80:83], v[120:123], v[44:47]\n"
"v_mfma_f32_16x16x32_bf16 v[44:47], a[92:95], v[124:127], v[44:47]\n"
"v_mfma_f32_16x16x32_bf16 v[44:47], a[104:107], v[128:131], v[44:47]\n"
"v_mfma_f32_16x16x32_bf16 v[48:51], a[60:63], v[128:131], v[48:51]\n"
"v_mfma_f32_16x16x32_bf16 v[48:51], a[48:51], v[124:127], v[48:51]\n"
"v_mfma_f32_16x16x32_bf16 v[48:51], a[36:39], v[120:123], v[48:51]\n"
"v_mfma_f32_16x16x32_bf16 v[52:55], a[40:43], v[120:123], v[52:55]\n"
"v_mfma_f32_16x16x32_bf16 v[52:55], a[52:55], v[124:127], v[52:55]\n"
"v_mfma_f32_16x16x32_bf16 v[52:55], a[64:67], v[128:131], v[52:55]\n"
"buffer_store_dword v239, v225, s[24:27], s38 offen offset:2816 sc1\n"
"v_mfma_f32_16x16x32_bf16 v[56:59], a[68:71], v[128:131], v[56:59]\n"
"v_mfma_f32_16x16x32_bf16 v[56:59], a[56:59], v[124:127], v[56:59]\n"
"v_mfma_f32_16x16x32_bf16 v[56:59], a[44:47], v[120:123], v[56:59]\n"
"v_mfma_f32_16x16x32_bf16 v[60:63], a[0:3], v[120:123], v[132:135]\n"
"v_mfma_f32_16x16x32_bf16 v[60:63], a[12:15], v[124:127], v[60:63]\n"
"v_mfma_f32_16x16x32_bf16 v[60:63], a[24:27], v[128:131], v[60:63]\n"
"v_mfma_f32_16x16x32_bf16 v[64:67], a[28:31], v[128:131], v[136:139]\n"
"v_mfma_f32_16x16x32_bf16 v[64:67], a[16:19], v[124:127], v[64:67]\n"
"v_mfma_f32_16x16x32_bf16 v[64:67], a[4:7], v[120:123], v[64:67]\n"
"v_mfma_f32_16x16x32_bf16 v[68:71], a[8:11], v[120:123], v[140:143]\n"
"v_mfma_f32_16x16x32_bf16 v[68:71], a[20:23], v[124:127], v[68:71]\n"
"buffer_store_dword v240, v225, s[24:27], s38 offen offset:3072 sc1\n"
"v_mfma_f32_16x16x32_bf16 v[68:71], a[32:35], v[128:131], v[68:71]\n"
"ds_read_b128 v[120:123], v213 offset:768\n"
"s_waitcnt lgkmcnt(7)\n"
"v_mfma_f32_16x16x32_bf16 v[84:87], v[176:179], v[108:111], v[84:87]\n"
"ds_read_b128 v[124:127], v213 offset:832\n"
"v_mfma_f32_16x16x32_bf16 v[84:87], v[188:191], v[112:115], v[84:87]\n"
"ds_read_b128 v[128:131], v213 offset:896\n"
"v_mfma_f32_16x16x32_bf16 v[84:87], v[200:203], v[116:119], v[84:87]\n"
"v_mfma_f32_16x16x32_bf16 v[88:91], v[204:207], v[116:119], v[88:91]\n"
"v_mfma_f32_16x16x32_bf16 v[88:91], v[192:195], v[112:115], v[88:91]\n"
"v_mfma_f32_16x16x32_bf16 v[88:91], v[180:183], v[108:111], v[88:91]\n"
"v_mfma_f32_16x16x32_bf16 v[92:95], v[184:187], v[108:111], v[92:95]\n"
"v_mfma_f32_16x16x32_bf16 v[92:95], v[196:199], v[112:115], v[92:95]\n"
"v_mfma_f32_16x16x32_bf16 v[92:95], v[208:211], v[116:119], v[92:95]\n"
"v_mfma_f32_16x16x32_bf16 v[96:99], v[164:167], v[116:119], v[96:99]\n"
"buffer_store_dword v241, v225, s[24:27], s38 offen offset:3328 sc1\n"
"v_mfma_f32_16x16x32_bf16 v[96:99], v[152:155], v[112:115], v[96:99]\n"
"v_cvt_pk_bf16_f32 v84, v84, v85\n"
"v_cvt_pk_bf16_f32 v85, v86, v87\n"
"v_mfma_f32_16x16x32_bf16 v[96:99], a[252:255], v[108:111], v[96:99]\n"
"ds_write_b32 v219, v84 offset:192\n"
"v_mfma_f32_16x16x32_bf16 v[100:103], v[144:147], v[108:111], v[100:103]\n"
"v_mfma_f32_16x16x32_bf16 v[100:103], v[156:159], v[112:115], v[100:103]\n"
"ds_write_b32 v219, v85 offset:196\n"
"v_mfma_f32_16x16x32_bf16 v[100:103], v[168:171], v[116:119], v[100:103]\n"
"v_cvt_pk_bf16_f32 v88, v88, v89\n"
"v_cvt_pk_bf16_f32 v89, v90, v91\n"
"v_mfma_f32_16x16x32_bf16 v[104:107], v[172:175], v[116:119], v[104:107]\n"
"ds_write_b32 v219, v88 offset:224\n"
"v_mfma_f32_16x16x32_bf16 v[104:107], v[160:163], v[112:115], v[104:107]\n"
"v_mfma_f32_16x16x32_bf16 v[104:107], v[148:151], v[108:111], v[104:107]\n"
"ds_write_b32 v219, v89 offset:228\n"
"v_mfma_f32_16x16x32_bf16 v[12:15], a[180:183], v[108:111], v[12:15]\n"
"v_cvt_pk_bf16_f32 v92, v92, v93\n"
"v_cvt_pk_bf16_f32 v93, v94, v95\n"
"v_mfma_f32_16x16x32_bf16 v[12:15], a[192:195], v[112:115], v[12:15]\n"
"ds_write_b32 v219, v92 offset:256\n"
"v_mfma_f32_16x16x32_bf16 v[12:15], a[204:207], v[116:119], v[12:15]\n"
"buffer_store_dword v242, v225, s[24:27], s38 offen offset:3584 sc1\n"
"v_mfma_f32_16x16x32_bf16 v[16:19], a[208:211], v[116:119], v[16:19]\n"
"ds_write_b32 v219, v93 offset:260\n"
"v_mfma_f32_16x16x32_bf16 v[16:19], a[196:199], v[112:115], v[16:19]\n"
"v_mfma_f32_16x16x32_bf16 v[16:19], a[184:187], v[108:111], v[16:19]\n"
"v_mfma_f32_16x16x32_bf16 v[20:23], a[188:191], v[108:111], v[20:23]\n"
"v_mfma_f32_16x16x32_bf16 v[20:23], a[200:203], v[112:115], v[20:23]\n"
"v_mfma_f32_16x16x32_bf16 v[20:23], a[212:215], v[116:119], v[20:23]\n"
"v_mfma_f32_16x16x32_bf16 v[24:27], a[168:171], v[116:119], v[24:27]\n"
"v_mfma_f32_16x16x32_bf16 v[24:27], a[156:159], v[112:115], v[24:27]\n"
"v_mfma_f32_16x16x32_bf16 v[24:27], a[144:147], v[108:111], v[24:27]\n"
"v_mfma_f32_16x16x32_bf16 v[28:31], a[148:151], v[108:111], v[28:31]\n"
"v_mfma_f32_16x16x32_bf16 v[28:31], a[160:163], v[112:115], v[28:31]\n"
"buffer_store_dword v243, v225, s[24:27], s38 offen offset:3840 sc1\n"
"v_mfma_f32_16x16x32_bf16 v[28:31], a[172:175], v[116:119], v[28:31]\n"
"v_mfma_f32_16x16x32_bf16 v[32:35], a[176:179], v[116:119], v[32:35]\n"
"v_mfma_f32_16x16x32_bf16 v[32:35], a[164:167], v[112:115], v[32:35]\n"
"v_mfma_f32_16x16x32_bf16 v[32:35], a[152:155], v[108:111], v[32:35]\n"
"v_mfma_f32_16x16x32_bf16 v[48:51], a[72:75], v[108:111], v[48:51]\n"
"v_mfma_f32_16x16x32_bf16 v[48:51], a[84:87], v[112:115], v[48:51]\n"
"v_mfma_f32_16x16x32_bf16 v[48:51], a[96:99], v[116:119], v[48:51]\n"
"v_mfma_f32_16x16x32_bf16 v[52:55], a[100:103], v[116:119], v[52:55]\n"
"v_mfma_f32_16x16x32_bf16 v[52:55], a[88:91], v[112:115], v[52:55]\n"
"v_mfma_f32_16x16x32_bf16 v[52:55], a[76:79], v[108:111], v[52:55]\n"
"v_mfma_f32_16x16x32_bf16 v[56:59], a[80:83], v[108:111], v[56:59]\n"
"buffer_store_dword v244, v226, s[24:27], s38 offen offset:0 sc1\n"
"v_mfma_f32_16x16x32_bf16 v[56:59], a[92:95], v[112:115], v[56:59]\n"
"v_mfma_f32_16x16x32_bf16 v[56:59], a[104:107], v[116:119], v[56:59]\n"
"v_mfma_f32_16x16x32_bf16 v[60:63], a[60:63], v[116:119], v[60:63]\n"
"v_mfma_f32_16x16x32_bf16 v[60:63], a[48:51], v[112:115], v[60:63]\n"
"v_mfma_f32_16x16x32_bf16 v[60:63], a[36:39], v[108:111], v[60:63]\n"
"v_mfma_f32_16x16x32_bf16 v[64:67], a[40:43], v[108:111], v[64:67]\n"
"v_mfma_f32_16x16x32_bf16 v[64:67], a[52:55], v[112:115], v[64:67]\n"
"v_mfma_f32_16x16x32_bf16 v[64:67], a[64:67], v[116:119], v[64:67]\n"
"v_mfma_f32_16x16x32_bf16 v[68:71], a[68:71], v[116:119], v[68:71]\n"
"v_mfma_f32_16x16x32_bf16 v[68:71], a[56:59], v[112:115], v[68:71]\n"
"v_mfma_f32_16x16x32_bf16 v[68:71], a[44:47], v[108:111], v[68:71]\n"
"v_add_u32 v212, 4294844416, v212\n"
"s_waitcnt lgkmcnt(6)\n"
"v_mfma_f32_16x16x32_bf16 v[96:99], v[176:179], v[120:123], v[96:99]\n"
"buffer_store_dword v245, v226, s[24:27], s38 offen offset:256 sc1\n"
"v_mfma_f32_16x16x32_bf16 v[96:99], v[188:191], v[124:127], v[96:99]\n"
"ds_read_b128 v[108:111], v212 offset:0\n"
"v_mfma_f32_16x16x32_bf16 v[96:99], v[200:203], v[128:131], v[96:99]\n"
"ds_read_b128 v[112:115], v212 offset:64\n"
"v_mfma_f32_16x16x32_bf16 v[100:103], v[204:207], v[128:131], v[100:103]\n"
"ds_read_b128 v[116:119], v212 offset:128\n"
"v_mfma_f32_16x16x32_bf16 v[100:103], v[192:195], v[124:127], v[100:103]\n"
"v_mfma_f32_16x16x32_bf16 v[100:103], v[180:183], v[120:123], v[100:103]\n"
"v_mfma_f32_16x16x32_bf16 v[104:107], v[184:187], v[120:123], v[104:107]\n"
"v_mfma_f32_16x16x32_bf16 v[104:107], v[196:199], v[124:127], v[104:107]\n"
"v_mfma_f32_16x16x32_bf16 v[104:107], v[208:211], v[128:131], v[104:107]\n"
"v_mfma_f32_16x16x32_bf16 v[24:27], a[204:207], v[128:131], v[24:27]\n"
"v_mfma_f32_16x16x32_bf16 v[24:27], a[192:195], v[124:127], v[24:27]\n"
"v_cvt_pk_bf16_f32 v96, v96, v97\n"
"v_cvt_pk_bf16_f32 v97, v98, v99\n"
"v_mfma_f32_16x16x32_bf16 v[24:27], a[180:183], v[120:123], v[24:27]\n"
"ds_write_b32 v219, v96 offset:384\n"
"v_mfma_f32_16x16x32_bf16 v[28:31], a[184:187], v[120:123], v[28:31]\n"
"v_mfma_f32_16x16x32_bf16 v[28:31], a[196:199], v[124:127], v[28:31]\n"
"ds_write_b32 v219, v97 offset:388\n"
"v_mfma_f32_16x16x32_bf16 v[28:31], a[208:211], v[128:131], v[28:31]\n"
"v_cvt_pk_bf16_f32 v100, v100, v101\n"
"v_cvt_pk_bf16_f32 v101, v102, v103\n"
"v_mfma_f32_16x16x32_bf16 v[32:35], a[212:215], v[128:131], v[32:35]\n"
"ds_write_b32 v219, v100 offset:416\n"
"v_mfma_f32_16x16x32_bf16 v[32:35], a[200:203], v[124:127], v[32:35]\n"
"v_mfma_f32_16x16x32_bf16 v[32:35], a[188:191], v[120:123], v[32:35]\n"
"ds_write_b32 v219, v101 offset:420\n"
"v_mfma_f32_16x16x32_bf16 v[60:63], a[72:75], v[120:123], v[60:63]\n"
"v_cvt_pk_bf16_f32 v104, v104, v105\n"
"v_cvt_pk_bf16_f32 v105, v106, v107\n"
"v_mfma_f32_16x16x32_bf16 v[60:63], a[84:87], v[124:127], v[60:63]\n"
"ds_write_b32 v219, v104 offset:448\n"
"v_mfma_f32_16x16x32_bf16 v[60:63], a[96:99], v[128:131], v[60:63]\n"
"v_mfma_f32_16x16x32_bf16 v[64:67], a[100:103], v[128:131], v[64:67]\n"
"ds_write_b32 v219, v105 offset:452\n"
"v_mfma_f32_16x16x32_bf16 v[64:67], a[88:91], v[124:127], v[64:67]\n"
"v_mfma_f32_16x16x32_bf16 v[64:67], a[76:79], v[120:123], v[64:67]\n"
"v_mfma_f32_16x16x32_bf16 v[68:71], a[80:83], v[120:123], v[68:71]\n"
"v_mfma_f32_16x16x32_bf16 v[68:71], a[92:95], v[124:127], v[68:71]\n"
"v_mfma_f32_16x16x32_bf16 v[68:71], a[104:107], v[128:131], v[68:71]\n"
"ds_read_b128 v[120:123], v212 offset:192\n"
"s_waitcnt lgkmcnt(7)\n"
"v_mfma_f32_16x16x32_bf16 v[0:3], a[216:219], v[108:111], v[0:3]\n"
"s_add_u32 s38, s38, s37\n"
"s_waitcnt vmcnt(32) lgkmcnt(1)\n"
"s_barrier\n"
"v_mfma_f32_16x16x32_bf16 v[0:3], a[228:231], v[112:115], v[0:3]\n"
"ds_read_b128 v[124:127], v212 offset:256\n"
"v_mfma_f32_16x16x32_bf16 v[0:3], a[240:243], v[116:119], v[0:3]\n"
"ds_read_b128 v[128:131], v212 offset:320\n"
"v_mfma_f32_16x16x32_bf16 v[4:7], a[244:247], v[116:119], v[4:7]\n"
"ds_read_b32 v228, v220 offset:0\n"
"v_mfma_f32_16x16x32_bf16 v[4:7], a[232:235], v[112:115], v[4:7]\n"
"ds_read_b32 v229, v220 offset:256\n"
"v_mfma_f32_16x16x32_bf16 v[4:7], a[220:223], v[108:111], v[4:7]\n"
"ds_read_b32 v230, v221 offset:512\n"
"v_mfma_f32_16x16x32_bf16 v[8:11], a[224:227], v[108:111], v[8:11]\n"
"ds_read_b32 v231, v220 offset:784\n"
"v_mfma_f32_16x16x32_bf16 v[8:11], a[236:239], v[112:115], v[8:11]\n"
"ds_read_b32 v232, v222 offset:1040\n"
"v_mfma_f32_16x16x32_bf16 v[8:11], a[248:251], v[116:119], v[8:11]\n"
"ds_read_b32 v233, v220 offset:1312\n"
"v_mfma_f32_16x16x32_bf16 v[36:39], a[132:135], v[116:119], v[36:39]\n"
"ds_read_b32 v234, v223 offset:1568\n"
"v_mfma_f32_16x16x32_bf16 v[36:39], a[120:123], v[112:115], v[36:39]\n"
"ds_read_b32 v235, v220 offset:1840\n"
"v_mfma_f32_16x16x32_bf16 v[36:39], a[108:111], v[108:111], v[36:39]\n"
"ds_read_b32 v236, v220 offset:2096\n"
"v_mfma_f32_16x16x32_bf16 v[40:43], a[112:115], v[108:111], v[40:43]\n"
"ds_read_b32 v237, v220 offset:2368\n"
"v_mfma_f32_16x16x32_bf16 v[40:43], a[124:127], v[112:115], v[40:43]\n"
"ds_read_b32 v238, v220 offset:2624\n"
"v_mfma_f32_16x16x32_bf16 v[40:43], a[136:139], v[116:119], v[40:43]\n"
"ds_read_b32 v239, v221 offset:2880\n"
"v_mfma_f32_16x16x32_bf16 v[44:47], a[140:143], v[116:119], v[44:47]\n"
"ds_read_b32 v240, v220 offset:3152\n"
"v_mfma_f32_16x16x32_bf16 v[44:47], a[128:131], v[112:115], v[44:47]\n"
"ds_read_b32 v241, v222 offset:3408\n"
"v_mfma_f32_16x16x32_bf16 v[44:47], a[116:119], v[108:111], v[44:47]\n"
"ds_read_b32 v242, v220 offset:3680\n"
"v_mfma_f32_16x16x32_bf16 v[72:75], a[0:3], v[108:111], v[132:135]\n"
"ds_read_b32 v243, v223 offset:3936\n"
"v_mfma_f32_16x16x32_bf16 v[72:75], a[12:15], v[112:115], v[72:75]\n"
"ds_read_b32 v244, v220 offset:4208\n"
"v_mfma_f32_16x16x32_bf16 v[72:75], a[24:27], v[116:119], v[72:75]\n"
"ds_read_b32 v245, v220 offset:4464\n"
"v_mfma_f32_16x16x32_bf16 v[76:79], a[28:31], v[116:119], v[136:139]\n"
"v_mfma_f32_16x16x32_bf16 v[76:79], a[16:19], v[112:115], v[76:79]\n"
"v_mfma_f32_16x16x32_bf16 v[76:79], a[4:7], v[108:111], v[76:79]\n"
"v_mfma_f32_16x16x32_bf16 v[80:83], a[8:11], v[108:111], v[140:143]\n"
"v_mfma_f32_16x16x32_bf16 v[80:83], a[20:23], v[112:115], v[80:83]\n"
"s_waitcnt lgkmcnt(0)\n"
"v_mfma_f32_16x16x32_bf16 v[80:83], a[32:35], v[116:119], v[80:83]\n"
"ds_read_b128 v[108:111], v212 offset:384\n"
"v_mfma_f32_16x16x32_bf16 v[0:3], a[252:255], v[120:123], v[0:3]\n"
"buffer_store_dword v228, v225, s[24:27], s38 offen offset:0 sc1\n"
"v_mfma_f32_16x16x32_bf16 v[0:3], v[152:155], v[124:127], v[0:3]\n"
"ds_read_b128 v[112:115], v212 offset:448\n"
"s_movk_i32 s47, 72\n"
"v_mfma_f32_16x16x32_bf16 v[0:3], v[164:167], v[128:131], v[0:3]\n"
"ds_read_b128 v[116:119], v212 offset:512\n"
"v_mbcnt_lo_u32_b32 v253, -1, 0\n"
"v_mfma_f32_16x16x32_bf16 v[4:7], v[168:171], v[128:131], v[4:7]\n"
"v_mbcnt_hi_u32_b32 v253, -1, v253\n"
"v_lshrrev_b32 v254, 5, v253\n"
"v_mfma_f32_16x16x32_bf16 v[4:7], v[156:159], v[124:127], v[4:7]\n"
"v_and_b32 v251, 15, v253\n"
"v_mad_u32_u24 v251, v254, s47, v251\n"
"v_mfma_f32_16x16x32_bf16 v[4:7], v[144:147], v[120:123], v[4:7]\n"
"v_bfe_u32 v252, v253, 4, 1\n"
"v_mad_u32_u24 v255, v252, 40, v251\n"
"v_mfma_f32_16x16x32_bf16 v[8:11], v[148:151], v[120:123], v[8:11]\n"
"v_lshl_add_u32 v214, v255, 4, s29\n"
"v_mul_u32_u24 v254, 57, v255\n"
"v_mfma_f32_16x16x32_bf16 v[8:11], v[160:163], v[124:127], v[8:11]\n"
"v_lshrrev_b32 v254, 11, v254\n"
"v_add_u32 v254, v254, v255\n"
"v_mfma_f32_16x16x32_bf16 v[8:11], v[172:175], v[128:131], v[8:11]\n"
"v_lshl_add_u32 v248, v254, 4, s28\n"
"v_bfe_u32 v254, v253, 3, 2\n"
"v_mfma_f32_16x16x32_bf16 v[12:15], a[240:243], v[128:131], v[12:15]\n"
"v_and_or_b32 v255, v253, 7, 16\n"
"v_mad_u32_u24 v255, v254, s47, v255\n"
"v_mfma_f32_16x16x32_bf16 v[12:15], a[228:231], v[124:127], v[12:15]\n"
"v_lshl_add_u32 v215, v255, 4, s29\n"
"v_mul_u32_u24 v254, 57, v255\n"
"v_mfma_f32_16x16x32_bf16 v[12:15], a[216:219], v[120:123], v[12:15]\n"
"buffer_store_dword v229, v225, s[24:27], s38 offen offset:256 sc1\n"
"v_mfma_f32_16x16x32_bf16 v[16:19], a[220:223], v[120:123], v[16:19]\n"
"v_lshrrev_b32 v254, 11, v254\n"
"v_add_u32 v254, v254, v255\n"
"v_mfma_f32_16x16x32_bf16 v[16:19], a[232:235], v[124:127], v[16:19]\n"
"v_lshl_add_u32 v249, v254, 4, s28\n"
"v_lshrrev_b32 v254, 5, v253\n"
"v_mfma_f32_16x16x32_bf16 v[16:19], a[244:247], v[128:131], v[16:19]\n"
"v_lshl_or_b32 v215, v254, 31, v215\n"
"v_lshl_add_u32 v255, v252, 5, v251\n"
"v_mfma_f32_16x16x32_bf16 v[20:23], a[248:251], v[128:131], v[20:23]\n"
"v_add_u32 v255, 24, v255\n"
"v_lshl_add_u32 v216, v255, 4, s29\n"
"v_mfma_f32_16x16x32_bf16 v[20:23], a[236:239], v[124:127], v[20:23]\n"
"v_mul_u32_u24 v254, 57, v255\n"
"v_lshrrev_b32 v254, 11, v254\n"
"v_mfma_f32_16x16x32_bf16 v[20:23], a[224:227], v[120:123], v[20:23]\n"
"v_add_u32 v254, v254, v255\n"
"v_lshl_add_u32 v250, v254, 4, s28\n"
"v_mfma_f32_16x16x32_bf16 v[36:39], a[144:147], v[120:123], v[36:39]\n"
"v_mfma_f32_16x16x32_bf16 v[36:39], a[156:159], v[124:127], v[36:39]\n"
"v_mfma_f32_16x16x32_bf16 v[36:39], a[168:171], v[128:131], v[36:39]\n"
"v_mfma_f32_16x16x32_bf16 v[40:43], a[172:175], v[128:131], v[40:43]\n"
"v_mfma_f32_16x16x32_bf16 v[40:43], a[160:163], v[124:127], v[40:43]\n"
"buffer_store_dword v230, v225, s[24:27], s38 offen offset:512 sc1\n"
"v_mfma_f32_16x16x32_bf16 v[40:43], a[148:151], v[120:123], v[40:43]\n"
"v_mfma_f32_16x16x32_bf16 v[44:47], a[152:155], v[120:123], v[44:47]\n"
"v_mfma_f32_16x16x32_bf16 v[44:47], a[164:167], v[124:127], v[44:47]\n"
"v_mfma_f32_16x16x32_bf16 v[44:47], a[176:179], v[128:131], v[44:47]\n"
"v_mfma_f32_16x16x32_bf16 v[48:51], a[132:135], v[128:131], v[48:51]\n"
"v_mfma_f32_16x16x32_bf16 v[48:51], a[120:123], v[124:127], v[48:51]\n"
"v_mfma_f32_16x16x32_bf16 v[48:51], a[108:111], v[120:123], v[48:51]\n"
"v_mfma_f32_16x16x32_bf16 v[52:55], a[112:115], v[120:123], v[52:55]\n"
"v_mfma_f32_16x16x32_bf16 v[52:55], a[124:127], v[124:127], v[52:55]\n"
"v_mfma_f32_16x16x32_bf16 v[52:55], a[136:139], v[128:131], v[52:55]\n"
"v_mfma_f32_16x16x32_bf16 v[56:59], a[140:143], v[128:131], v[56:59]\n"
"buffer_store_dword v231, v225, s[24:27], s38 offen offset:768 sc1\n"
"v_mfma_f32_16x16x32_bf16 v[56:59], a[128:131], v[124:127], v[56:59]\n"
"v_mfma_f32_16x16x32_bf16 v[56:59], a[116:119], v[120:123], v[56:59]\n"
"v_mfma_f32_16x16x32_bf16 v[72:75], a[36:39], v[120:123], v[72:75]\n"
"v_mfma_f32_16x16x32_bf16 v[72:75], a[48:51], v[124:127], v[72:75]\n"
"v_mfma_f32_16x16x32_bf16 v[72:75], a[60:63], v[128:131], v[72:75]\n"
"v_mfma_f32_16x16x32_bf16 v[76:79], a[64:67], v[128:131], v[76:79]\n"
"v_mfma_f32_16x16x32_bf16 v[76:79], a[52:55], v[124:127], v[76:79]\n"
"v_mfma_f32_16x16x32_bf16 v[76:79], a[40:43], v[120:123], v[76:79]\n"
"v_mfma_f32_16x16x32_bf16 v[80:83], a[44:47], v[120:123], v[80:83]\n"
"v_mfma_f32_16x16x32_bf16 v[80:83], a[56:59], v[124:127], v[80:83]\n"
"v_mfma_f32_16x16x32_bf16 v[80:83], a[68:71], v[128:131], v[80:83]\n"
"buffer_store_dword v232, v225, s[24:27], s38 offen offset:1024 sc1\n"
"v_mfma_f32_16x16x32_bf16 v[84:87], a[24:27], v[128:131], v[132:135]\n"
"v_mfma_f32_16x16x32_bf16 v[84:87], a[12:15], v[124:127], v[84:87]\n"
"v_mfma_f32_16x16x32_bf16 v[84:87], a[0:3], v[120:123], v[84:87]\n"
"v_mfma_f32_16x16x32_bf16 v[88:91], a[4:7], v[120:123], v[136:139]\n"
"v_mfma_f32_16x16x32_bf16 v[88:91], a[16:19], v[124:127], v[88:91]\n"
"v_mfma_f32_16x16x32_bf16 v[88:91], a[28:31], v[128:131], v[88:91]\n"
"v_mfma_f32_16x16x32_bf16 v[92:95], a[32:35], v[128:131], v[140:143]\n"
"v_mfma_f32_16x16x32_bf16 v[92:95], a[20:23], v[124:127], v[92:95]\n"
"v_mfma_f32_16x16x32_bf16 v[92:95], a[8:11], v[120:123], v[92:95]\n"
"v_add_u32 v213, 4294844416, v213\n"
"s_waitcnt lgkmcnt(0)\n"
"v_mfma_f32_16x16x32_bf16 v[0:3], v[176:179], v[108:111], v[0:3]\n"
"ds_read_b128 v[120:123], v213 offset:576\n"
"v_mfma_f32_16x16x32_bf16 v[0:3], v[188:191], v[112:115], v[0:3]\n"
"buffer_store_dword v233, v225, s[24:27], s38 offen offset:1280 sc1\n"
"v_mfma_f32_16x16x32_bf16 v[0:3], v[200:203], v[116:119], v[0:3]\n"
"ds_read_b128 v[124:127], v213 offset:640\n"
"v_mfma_f32_16x16x32_bf16 v[4:7], v[204:207], v[116:119], v[4:7]\n"
"ds_read_b128 v[128:131], v213 offset:704\n"
"v_mfma_f32_16x16x32_bf16 v[4:7], v[192:195], v[112:115], v[4:7]\n"
"v_mfma_f32_16x16x32_bf16 v[4:7], v[180:183], v[108:111], v[4:7]\n"
"v_mfma_f32_16x16x32_bf16 v[8:11], v[184:187], v[108:111], v[8:11]\n"
"v_mfma_f32_16x16x32_bf16 v[8:11], v[196:199], v[112:115], v[8:11]\n"
"v_mfma_f32_16x16x32_bf16 v[8:11], v[208:211], v[116:119], v[8:11]\n"
"v_mfma_f32_16x16x32_bf16 v[12:15], v[164:167], v[116:119], v[12:15]\n"
"v_mfma_f32_16x16x32_bf16 v[12:15], v[152:155], v[112:115], v[12:15]\n"
"v_cvt_pk_bf16_f32 v0, v0, v1\n"
"v_cvt_pk_bf16_f32 v1, v2, v3\n"
"v_mfma_f32_16x16x32_bf16 v[12:15], a[252:255], v[108:111], v[12:15]\n"
"ds_write_b32 v219, v0 offset:59904\n"
"v_mfma_f32_16x16x32_bf16 v[16:19], v[144:147], v[108:111], v[16:19]\n"
"buffer_store_dword v234, v225, s[24:27], s38 offen offset:1536 sc1\n"
"v_mfma_f32_16x16x32_bf16 v[16:19], v[156:159], v[112:115], v[16:19]\n"
"ds_write_b32 v219, v1 offset:59908\n"
"v_mfma_f32_16x16x32_bf16 v[16:19], v[168:171], v[116:119], v[16:19]\n"
"v_cvt_pk_bf16_f32 v4, v4, v5\n"
"v_cvt_pk_bf16_f32 v5, v6, v7\n"
"v_mfma_f32_16x16x32_bf16 v[20:23], v[172:175], v[116:119], v[20:23]\n"
"ds_write_b32 v219, v4 offset:59936\n"
"v_mfma_f32_16x16x32_bf16 v[20:23], v[160:163], v[112:115], v[20:23]\n"
"v_mfma_f32_16x16x32_bf16 v[20:23], v[148:151], v[108:111], v[20:23]\n"
"ds_write_b32 v219, v5 offset:59940\n"
"v_mfma_f32_16x16x32_bf16 v[24:27], a[216:219], v[108:111], v[24:27]\n"
"v_cvt_pk_bf16_f32 v8, v8, v9\n"
"v_cvt_pk_bf16_f32 v9, v10, v11\n"
"v_mfma_f32_16x16x32_bf16 v[24:27], a[228:231], v[112:115], v[24:27]\n"
"ds_write_b32 v219, v8 offset:59968\n"
"v_mfma_f32_16x16x32_bf16 v[24:27], a[240:243], v[116:119], v[24:27]\n"
"v_mfma_f32_16x16x32_bf16 v[28:31], a[244:247], v[116:119], v[28:31]\n"
"ds_write_b32 v219, v9 offset:59972\n"
"v_mfma_f32_16x16x32_bf16 v[28:31], a[232:235], v[112:115], v[28:31]\n"
"v_mfma_f32_16x16x32_bf16 v[28:31], a[220:223], v[108:111], v[28:31]\n"
"buffer_store_dword v235, v225, s[24:27], s38 offen offset:1792 sc1\n"
"v_mfma_f32_16x16x32_bf16 v[32:35], a[224:227], v[108:111], v[32:35]\n"
"v_mfma_f32_16x16x32_bf16 v[32:35], a[236:239], v[112:115], v[32:35]\n"
"v_mfma_f32_16x16x32_bf16 v[32:35], a[248:251], v[116:119], v[32:35]\n"
"v_mfma_f32_16x16x32_bf16 v[36:39], a[204:207], v[116:119], v[36:39]\n"
"v_mfma_f32_16x16x32_bf16 v[36:39], a[192:195], v[112:115], v[36:39]\n"
"v_mfma_f32_16x16x32_bf16 v[36:39], a[180:183], v[108:111], v[36:39]\n"
"v_mfma_f32_16x16x32_bf16 v[40:43], a[184:187], v[108:111], v[40:43]\n"
"v_mfma_f32_16x16x32_bf16 v[40:43], a[196:199], v[112:115], v[40:43]\n"
"v_mfma_f32_16x16x32_bf16 v[40:43], a[208:211], v[116:119], v[40:43]\n"
"v_mfma_f32_16x16x32_bf16 v[44:47], a[212:215], v[116:119], v[44:47]\n"
"v_mfma_f32_16x16x32_bf16 v[44:47], a[200:203], v[112:115], v[44:47]\n"
"buffer_store_dword v236, v225, s[24:27], s38 offen offset:2048 sc1\n"
"v_mfma_f32_16x16x32_bf16 v[44:47], a[188:191], v[108:111], v[44:47]\n"
"v_mfma_f32_16x16x32_bf16 v[48:51], a[144:147], v[108:111], v[48:51]\n"
"v_mfma_f32_16x16x32_bf16 v[48:51], a[156:159], v[112:115], v[48:51]\n"
"v_mfma_f32_16x16x32_bf16 v[48:51], a[168:171], v[116:119], v[48:51]\n"
"v_mfma_f32_16x16x32_bf16 v[52:55], a[172:175], v[116:119], v[52:55]\n"
"v_mfma_f32_16x16x32_bf16 v[52:55], a[160:163], v[112:115], v[52:55]\n"
"v_mfma_f32_16x16x32_bf16 v[52:55], a[148:151], v[108:111], v[52:55]\n"
"v_mfma_f32_16x16x32_bf16 v[56:59], a[152:155], v[108:111], v[56:59]\n"
"v_mfma_f32_16x16x32_bf16 v[56:59], a[164:167], v[112:115], v[56:59]\n"
"v_mfma_f32_16x16x32_bf16 v[56:59], a[176:179], v[116:119], v[56:59]\n"
"v_mfma_f32_16x16x32_bf16 v[60:63], a[132:135], v[116:119], v[60:63]\n"
"buffer_store_dword v237, v225, s[24:27], s38 offen offset:2304 sc1\n"
"v_mfma_f32_16x16x32_bf16 v[60:63], a[120:123], v[112:115], v[60:63]\n"
"v_mfma_f32_16x16x32_bf16 v[60:63], a[108:111], v[108:111], v[60:63]\n"
"v_mfma_f32_16x16x32_bf16 v[64:67], a[112:115], v[108:111], v[64:67]\n"
"v_mfma_f32_16x16x32_bf16 v[64:67], a[124:127], v[112:115], v[64:67]\n"
"v_mfma_f32_16x16x32_bf16 v[64:67], a[136:139], v[116:119], v[64:67]\n"
"v_mfma_f32_16x16x32_bf16 v[68:71], a[140:143], v[116:119], v[68:71]\n"
"v_mfma_f32_16x16x32_bf16 v[68:71], a[128:131], v[112:115], v[68:71]\n"
"v_mfma_f32_16x16x32_bf16 v[68:71], a[116:119], v[108:111], v[68:71]\n"
"v_mfma_f32_16x16x32_bf16 v[72:75], a[72:75], v[108:111], v[72:75]\n"
"v_mfma_f32_16x16x32_bf16 v[72:75], a[84:87], v[112:115], v[72:75]\n"
"v_mfma_f32_16x16x32_bf16 v[72:75], a[96:99], v[116:119], v[72:75]\n"
"buffer_store_dword v238, v225, s[24:27], s38 offen offset:2560 sc1\n"
"v_mfma_f32_16x16x32_bf16 v[76:79], a[100:103], v[116:119], v[76:79]\n"
"v_mfma_f32_16x16x32_bf16 v[76:79], a[88:91], v[112:115], v[76:79]\n"
"v_mfma_f32_16x16x32_bf16 v[76:79], a[76:79], v[108:111], v[76:79]\n"
"v_mfma_f32_16x16x32_bf16 v[80:83], a[80:83], v[108:111], v[80:83]\n"
"v_mfma_f32_16x16x32_bf16 v[80:83], a[92:95], v[112:115], v[80:83]\n"
"v_mfma_f32_16x16x32_bf16 v[80:83], a[104:107], v[116:119], v[80:83]\n"
"v_mfma_f32_16x16x32_bf16 v[84:87], a[60:63], v[116:119], v[84:87]\n"
"v_mfma_f32_16x16x32_bf16 v[84:87], a[48:51], v[112:115], v[84:87]\n"
"v_mfma_f32_16x16x32_bf16 v[84:87], a[36:39], v[108:111], v[84:87]\n"
"v_mfma_f32_16x16x32_bf16 v[88:91], a[40:43], v[108:111], v[88:91]\n"
"v_mfma_f32_16x16x32_bf16 v[88:91], a[52:55], v[112:115], v[88:91]\n"
"buffer_store_dword v239, v225, s[24:27], s38 offen offset:2816 sc1\n"
"v_mfma_f32_16x16x32_bf16 v[88:91], a[64:67], v[116:119], v[88:91]\n"
"v_mfma_f32_16x16x32_bf16 v[92:95], a[68:71], v[116:119], v[92:95]\n"
"v_mfma_f32_16x16x32_bf16 v[92:95], a[56:59], v[112:115], v[92:95]\n"
"v_mfma_f32_16x16x32_bf16 v[92:95], a[44:47], v[108:111], v[92:95]\n"
"v_mfma_f32_16x16x32_bf16 v[96:99], a[0:3], v[108:111], v[132:135]\n"
"v_mfma_f32_16x16x32_bf16 v[96:99], a[12:15], v[112:115], v[96:99]\n"
"v_mfma_f32_16x16x32_bf16 v[96:99], a[24:27], v[116:119], v[96:99]\n"
"v_mfma_f32_16x16x32_bf16 v[100:103], a[28:31], v[116:119], v[136:139]\n"
"v_mfma_f32_16x16x32_bf16 v[100:103], a[16:19], v[112:115], v[100:103]\n"
"v_mfma_f32_16x16x32_bf16 v[100:103], a[4:7], v[108:111], v[100:103]\n"
"v_mfma_f32_16x16x32_bf16 v[104:107], a[8:11], v[108:111], v[140:143]\n"
"buffer_store_dword v240, v225, s[24:27], s38 offen offset:3072 sc1\n"
"v_mfma_f32_16x16x32_bf16 v[104:107], a[20:23], v[112:115], v[104:107]\n"
"v_mfma_f32_16x16x32_bf16 v[104:107], a[32:35], v[116:119], v[104:107]\n"
"ds_read_b128 v[108:111], v213 offset:768\n"
"s_waitcnt lgkmcnt(7)\n"
"v_mfma_f32_16x16x32_bf16 v[12:15], v[176:179], v[120:123], v[12:15]\n"
"ds_read_b128 v[112:115], v213 offset:832\n"
"v_mfma_f32_16x16x32_bf16 v[12:15], v[188:191], v[124:127], v[12:15]\n"
"ds_read_b128 v[116:119], v213 offset:896\n"
"v_mfma_f32_16x16x32_bf16 v[12:15], v[200:203], v[128:131], v[12:15]\n"
"v_mfma_f32_16x16x32_bf16 v[16:19], v[204:207], v[128:131], v[16:19]\n"
"v_mfma_f32_16x16x32_bf16 v[16:19], v[192:195], v[124:127], v[16:19]\n"
"v_mfma_f32_16x16x32_bf16 v[16:19], v[180:183], v[120:123], v[16:19]\n"
"v_mfma_f32_16x16x32_bf16 v[20:23], v[184:187], v[120:123], v[20:23]\n"
"v_mfma_f32_16x16x32_bf16 v[20:23], v[196:199], v[124:127], v[20:23]\n"
"v_mfma_f32_16x16x32_bf16 v[20:23], v[208:211], v[128:131], v[20:23]\n"
"buffer_store_dword v241, v225, s[24:27], s38 offen offset:3328 sc1\n"
"v_mfma_f32_16x16x32_bf16 v[24:27], v[164:167], v[128:131], v[24:27]\n"
"v_mfma_f32_16x16x32_bf16 v[24:27], v[152:155], v[124:127], v[24:27]\n"
"v_cvt_pk_bf16_f32 v12, v12, v13\n"
"v_cvt_pk_bf16_f32 v13, v14, v15\n"
"v_mfma_f32_16x16x32_bf16 v[24:27], a[252:255], v[120:123], v[24:27]\n"
"ds_write_b32 v219, v12 offset:60096\n"
"v_mfma_f32_16x16x32_bf16 v[28:31], v[144:147], v[120:123], v[28:31]\n"
"v_mfma_f32_16x16x32_bf16 v[28:31], v[156:159], v[124:127], v[28:31]\n"
"ds_write_b32 v219, v13 offset:60100\n"
"v_mfma_f32_16x16x32_bf16 v[28:31], v[168:171], v[128:131], v[28:31]\n"
"v_cvt_pk_bf16_f32 v16, v16, v17\n"
"v_cvt_pk_bf16_f32 v17, v18, v19\n"
"v_mfma_f32_16x16x32_bf16 v[32:35], v[172:175], v[128:131], v[32:35]\n"
"ds_write_b32 v219, v16 offset:60128\n"
"v_mfma_f32_16x16x32_bf16 v[32:35], v[160:163], v[124:127], v[32:35]\n"
"v_mfma_f32_16x16x32_bf16 v[32:35], v[148:151], v[120:123], v[32:35]\n"
"ds_write_b32 v219, v17 offset:60132\n"
"v_mfma_f32_16x16x32_bf16 v[48:51], a[180:183], v[120:123], v[48:51]\n"
"v_cvt_pk_bf16_f32 v20, v20, v21\n"
"v_cvt_pk_bf16_f32 v21, v22, v23\n"
"v_mfma_f32_16x16x32_bf16 v[48:51], a[192:195], v[124:127], v[48:51]\n"
"buffer_store_dword v242, v225, s[24:27], s38 offen offset:3584 sc1\n"
"v_mfma_f32_16x16x32_bf16 v[48:51], a[204:207], v[128:131], v[48:51]\n"
"ds_write_b32 v219, v20 offset:60160\n"
"v_mfma_f32_16x16x32_bf16 v[52:55], a[208:211], v[128:131], v[52:55]\n"
"v_mfma_f32_16x16x32_bf16 v[52:55], a[196:199], v[124:127], v[52:55]\n"
"ds_write_b32 v219, v21 offset:60164\n"
"v_mfma_f32_16x16x32_bf16 v[52:55], a[184:187], v[120:123], v[52:55]\n"
"v_mfma_f32_16x16x32_bf16 v[56:59], a[188:191], v[120:123], v[56:59]\n"
"v_mfma_f32_16x16x32_bf16 v[56:59], a[200:203], v[124:127], v[56:59]\n"
"v_mfma_f32_16x16x32_bf16 v[56:59], a[212:215], v[128:131], v[56:59]\n"
"v_mfma_f32_16x16x32_bf16 v[60:63], a[168:171], v[128:131], v[60:63]\n"
"v_mfma_f32_16x16x32_bf16 v[60:63], a[156:159], v[124:127], v[60:63]\n"
"v_mfma_f32_16x16x32_bf16 v[60:63], a[144:147], v[120:123], v[60:63]\n"
"v_mfma_f32_16x16x32_bf16 v[64:67], a[148:151], v[120:123], v[64:67]\n"
"buffer_store_dword v243, v225, s[24:27], s38 offen offset:3840 sc1\n"
"v_mfma_f32_16x16x32_bf16 v[64:67], a[160:163], v[124:127], v[64:67]\n"
"v_mfma_f32_16x16x32_bf16 v[64:67], a[172:175], v[128:131], v[64:67]\n"
"v_mfma_f32_16x16x32_bf16 v[68:71], a[176:179], v[128:131], v[68:71]\n"
"v_mfma_f32_16x16x32_bf16 v[68:71], a[164:167], v[124:127], v[68:71]\n"
"v_mfma_f32_16x16x32_bf16 v[68:71], a[152:155], v[120:123], v[68:71]\n"
"v_mfma_f32_16x16x32_bf16 v[84:87], a[72:75], v[120:123], v[84:87]\n"
"v_mfma_f32_16x16x32_bf16 v[84:87], a[84:87], v[124:127], v[84:87]\n"
"v_mfma_f32_16x16x32_bf16 v[84:87], a[96:99], v[128:131], v[84:87]\n"
"v_mfma_f32_16x16x32_bf16 v[88:91], a[100:103], v[128:131], v[88:91]\n"
"v_mfma_f32_16x16x32_bf16 v[88:91], a[88:91], v[124:127], v[88:91]\n"
"v_mfma_f32_16x16x32_bf16 v[88:91], a[76:79], v[120:123], v[88:91]\n"
"buffer_store_dword v244, v226, s[24:27], s38 offen offset:0 sc1\n"
"v_mfma_f32_16x16x32_bf16 v[92:95], a[80:83], v[120:123], v[92:95]\n"
"v_mfma_f32_16x16x32_bf16 v[92:95], a[92:95], v[124:127], v[92:95]\n"
"v_mfma_f32_16x16x32_bf16 v[92:95], a[104:107], v[128:131], v[92:95]\n"
"v_mfma_f32_16x16x32_bf16 v[96:99], a[60:63], v[128:131], v[96:99]\n"
"v_mfma_f32_16x16x32_bf16 v[96:99], a[48:51], v[124:127], v[96:99]\n"
"v_mfma_f32_16x16x32_bf16 v[96:99], a[36:39], v[120:123], v[96:99]\n"
"v_mfma_f32_16x16x32_bf16 v[100:103], a[40:43], v[120:123], v[100:103]\n"
"v_mfma_f32_16x16x32_bf16 v[100:103], a[52:55], v[124:127], v[100:103]\n"
"v_mfma_f32_16x16x32_bf16 v[100:103], a[64:67], v[128:131], v[100:103]\n"
"v_mfma_f32_16x16x32_bf16 v[104:107], a[68:71], v[128:131], v[104:107]\n"
"v_mfma_f32_16x16x32_bf16 v[104:107], a[56:59], v[124:127], v[104:107]\n"
"buffer_store_dword v245, v226, s[24:27], s38 offen offset:256 sc1\n"
"v_mfma_f32_16x16x32_bf16 v[104:107], a[44:47], v[120:123], v[104:107]\n"
"v_add_u32 v212, 59904, v212\n"
"s_waitcnt lgkmcnt(6)\n"
".Lconv_stub3:\n"
"s_cbranch_vccnz .Lconv_ret\n"
"v_mfma_f32_16x16x32_bf16 v[24:27], v[176:179], v[108:111], v[24:27]\n"
"ds_read_b128 v[120:123], v212 offset:0\n"
"v_mfma_f32_16x16x32_bf16 v[24:27], v[188:191], v[112:115], v[24:27]\n"
"ds_read_b128 v[124:127], v212 offset:64\n"
"v_mfma_f32_16x16x32_bf16 v[24:27], v[200:203], v[116:119], v[24:27]\n"
"ds_read_b128 v[128:131], v212 offset:128\n"
"v_mfma_f32_16x16x32_bf16 v[28:31], v[204:207], v[116:119], v[28:31]\n"
"v_mfma_f32_16x16x32_bf16 v[28:31], v[192:195], v[112:115], v[28:31]\n"
"v_mfma_f32_16x16x32_bf16 v[28:31], v[180:183], v[108:111], v[28:31]\n"
"v_mfma_f32_16x16x32_bf16 v[32:35], v[184:187], v[108:111], v[32:35]\n"
"v_mfma_f32_16x16x32_bf16 v[32:35], v[196:199], v[112:115], v[32:35]\n"
"v_mfma_f32_16x16x32_bf16 v[32:35], v[208:211], v[116:119], v[32:35]\n"
"v_mfma_f32_16x16x32_bf16 v[60:63], a[204:207], v[116:119], v[60:63]\n"
"v_mfma_f32_16x16x32_bf16 v[60:63], a[192:195], v[112:115], v[60:63]\n"
"v_cvt_pk_bf16_f32 v24, v24, v25\n"
"v_cvt_pk_bf16_f32 v25, v26, v27\n"
"v_mfma_f32_16x16x32_bf16 v[60:63], a[180:183], v[108:111], v[60:63]\n"
"ds_write_b32 v219, v24 offset:60288\n"
"v_mfma_f32_16x16x32_bf16 v[64:67], a[184:187], v[108:111], v[64:67]\n"
"v_mfma_f32_16x16x32_bf16 v[64:67], a[196:199], v[112:115], v[64:67]\n"
"ds_write_b32 v219, v25 offset:60292\n"
"v_mfma_f32_16x16x32_bf16 v[64:67], a[208:211], v[116:119], v[64:67]\n"
"v_cvt_pk_bf16_f32 v28, v28, v29\n"
"v_cvt_pk_bf16_f32 v29, v30, v31\n"
"v_mfma_f32_16x16x32_bf16 v[68:71], a[212:215], v[116:119], v[68:71]\n"
"ds_write_b32 v219, v28 offset:60320\n"
"v_mfma_f32_16x16x32_bf16 v[68:71], a[200:203], v[112:115], v[68:71]\n"
"v_mfma_f32_16x16x32_bf16 v[68:71], a[188:191], v[108:111], v[68:71]\n"
"ds_write_b32 v219, v29 offset:60324\n"
"v_mfma_f32_16x16x32_bf16 v[96:99], a[72:75], v[108:111], v[96:99]\n"
"v_cvt_pk_bf16_f32 v32, v32, v33\n"
"v_cvt_pk_bf16_f32 v33, v34, v35\n"
"v_mfma_f32_16x16x32_bf16 v[96:99], a[84:87], v[112:115], v[96:99]\n"
"ds_write_b32 v219, v32 offset:60352\n"
"v_mfma_f32_16x16x32_bf16 v[96:99], a[96:99], v[116:119], v[96:99]\n"
"v_mfma_f32_16x16x32_bf16 v[100:103], a[100:103], v[116:119], v[100:103]\n"
"ds_write_b32 v219, v33 offset:60356\n"
"v_mfma_f32_16x16x32_bf16 v[100:103], a[88:91], v[112:115], v[100:103]\n"
"v_mfma_f32_16x16x32_bf16 v[100:103], a[76:79], v[108:111], v[100:103]\n"
"v_mfma_f32_16x16x32_bf16 v[104:107], a[80:83], v[108:111], v[104:107]\n"
"v_mfma_f32_16x16x32_bf16 v[104:107], a[92:95], v[112:115], v[104:107]\n"
"v_mfma_f32_16x16x32_bf16 v[104:107], a[104:107], v[116:119], v[104:107]\n"
"ds_read_b128 v[108:111], v212 offset:192\n"
"s_waitcnt lgkmcnt(7)\n"
"v_mfma_f32_16x16x32_bf16 v[36:39], a[216:219], v[120:123], v[36:39]\n"
"s_add_u32 s38, s38, s37\n"
"s_waitcnt vmcnt(33) lgkmcnt(1)\n"
"s_barrier\n"
"v_mfma_f32_16x16x32_bf16 v[36:39], a[228:231], v[124:127], v[36:39]\n"
"ds_read_b128 v[112:115], v212 offset:256\n"
"v_mfma_f32_16x16x32_bf16 v[36:39], a[240:243], v[128:131], v[36:39]\n"
"ds_read_b128 v[116:119], v212 offset:320\n"
"v_mfma_f32_16x16x32_bf16 v[40:43], a[244:247], v[128:131], v[40:43]\n"
"ds_read_b32 v228, v220 offset:59904\n"
"v_mfma_f32_16x16x32_bf16 v[40:43], a[232:235], v[124:127], v[40:43]\n"
"ds_read_b32 v229, v220 offset:60160\n"
"v_mfma_f32_16x16x32_bf16 v[40:43], a[220:223], v[120:123], v[40:43]\n"
"ds_read_b32 v230, v221 offset:60416\n"
"v_mfma_f32_16x16x32_bf16 v[44:47], a[224:227], v[120:123], v[44:47]\n"
"ds_read_b32 v231, v220 offset:60688\n"
"v_mfma_f32_16x16x32_bf16 v[44:47], a[236:239], v[124:127], v[44:47]\n"
"ds_read_b32 v232, v222 offset:60944\n"
"v_mfma_f32_16x16x32_bf16 v[44:47], a[248:251], v[128:131], v[44:47]\n"
"ds_read_b128 v[120:123], v212 offset:384\n"
"s_waitcnt lgkmcnt(6)\n"
"v_mfma_f32_16x16x32_bf16 v[36:39], a[252:255], v[108:111], v[36:39]\n"
"ds_read_b128 v[124:127], v212 offset:448\n"
"v_mfma_f32_16x16x32_bf16 v[36:39], v[152:155], v[112:115], v[36:39]\n"
"ds_read_b128 v[128:131], v212 offset:512\n"
"v_mfma_f32_16x16x32_bf16 v[36:39], v[164:167], v[116:119], v[36:39]\n"
"ds_read_b32 v233, v220 offset:61216\n"
"v_mfma_f32_16x16x32_bf16 v[40:43], v[168:171], v[116:119], v[40:43]\n"
"ds_read_b32 v234, v223 offset:61472\n"
"v_mfma_f32_16x16x32_bf16 v[40:43], v[156:159], v[112:115], v[40:43]\n"
"ds_read_b32 v235, v220 offset:61744\n"
"v_mfma_f32_16x16x32_bf16 v[40:43], v[144:147], v[108:111], v[40:43]\n"
"ds_read_b32 v236, v220 offset:62000\n"
"v_mfma_f32_16x16x32_bf16 v[44:47], v[148:151], v[108:111], v[44:47]\n"
"ds_read_b32 v237, v220 offset:62272\n"
"v_mfma_f32_16x16x32_bf16 v[44:47], v[160:163], v[112:115], v[44:47]\n"
"ds_read_b32 v238, v220 offset:62528\n"
"v_mfma_f32_16x16x32_bf16 v[44:47], v[172:175], v[116:119], v[44:47]\n"
"ds_read_b32 v239, v221 offset:62784\n"
"v_mfma_f32_16x16x32_bf16 v[48:51], a[240:243], v[116:119], v[48:51]\n"
"ds_read_b32 v240, v220 offset:63056\n"
"v_mfma_f32_16x16x32_bf16 v[48:51], a[228:231], v[112:115], v[48:51]\n"
"ds_read_b32 v241, v222 offset:63312\n"
"v_mfma_f32_16x16x32_bf16 v[48:51], a[216:219], v[108:111], v[48:51]\n"
"ds_read_b32 v242, v220 offset:63584\n"
"v_mfma_f32_16x16x32_bf16 v[52:55], a[220:223], v[108:111], v[52:55]\n"
"ds_read_b32 v243, v223 offset:63840\n"
"v_mfma_f32_16x16x32_bf16 v[52:55], a[232:235], v[112:115], v[52:55]\n"
"ds_read_b32 v244, v220 offset:64112\n"
"v_mfma_f32_16x16x32_bf16 v[52:55], a[244:247], v[116:119], v[52:55]\n"
"ds_read_b32 v245, v220 offset:64368\n"
"v_mfma_f32_16x16x32_bf16 v[56:59], a[248:251], v[116:119], v[56:59]\n"
"v_mfma_f32_16x16x32_bf16 v[56:59], a[236:239], v[112:115], v[56:59]\n"
"v_mfma_f32_16x16x32_bf16 v[56:59], a[224:227], v[108:111], v[56:59]\n"
"v_add_u32 v213, 59904, v213\n"
"s_waitcnt lgkmcnt(13)\n"
"v_mfma_f32_16x16x32_bf16 v[36:39], v[176:179], v[120:123], v[36:39]\n"
"ds_read_b128 v[108:111], v213 offset:576\n"
"v_mfma_f32_16x16x32_bf16 v[36:39], v[188:191], v[124:127], v[36:39]\n"
"ds_read_b128 v[112:115], v213 offset:640\n"
"s_waitcnt lgkmcnt(2)\n"
"v_mfma_f32_16x16x32_bf16 v[36:39], v[200:203], v[128:131], v[36:39]\n"
"buffer_store_dword v228, v225, s[24:27], s38 offen offset:0 sc1\n"
"v_mfma_f32_16x16x32_bf16 v[40:43], v[204:207], v[128:131], v[40:43]\n"
"ds_read_b128 v[116:119], v213 offset:704\n"
"v_mfma_f32_16x16x32_bf16 v[40:43], v[192:195], v[124:127], v[40:43]\n"
"buffer_store_dword v229, v225, s[24:27], s38 offen offset:256 sc1\n"
"v_mfma_f32_16x16x32_bf16 v[40:43], v[180:183], v[120:123], v[40:43]\n"
"ds_read_b128 v[0:3], v212 offset:384\n"
"v_mfma_f32_16x16x32_bf16 v[44:47], v[184:187], v[120:123], v[44:47]\n"
"buffer_store_dword v230, v225, s[24:27], s38 offen offset:512 sc1\n"
"v_mfma_f32_16x16x32_bf16 v[44:47], v[196:199], v[124:127], v[44:47]\n"
"ds_read_b128 v[4:7], v212 offset:448\n"
"v_mfma_f32_16x16x32_bf16 v[44:47], v[208:211], v[128:131], v[44:47]\n"
"buffer_store_dword v231, v225, s[24:27], s38 offen offset:768 sc1\n"
"v_mfma_f32_16x16x32_bf16 v[48:51], v[164:167], v[128:131], v[48:51]\n"
"ds_read_b128 v[8:11], v212 offset:512\n"
"v_mfma_f32_16x16x32_bf16 v[48:51], v[152:155], v[124:127], v[48:51]\n"
"buffer_store_dword v232, v225, s[24:27], s38 offen offset:1024 sc1\n"
"v_mfma_f32_16x16x32_bf16 v[48:51], a[252:255], v[120:123], v[48:51]\n"
"ds_read_b128 v[12:15], v213 offset:576\n"
"v_cvt_pk_bf16_f32 v36, v36, v37\n"
"v_mfma_f32_16x16x32_bf16 v[52:55], v[144:147], v[120:123], v[52:55]\n"
"buffer_store_dword v233, v225, s[24:27], s38 offen offset:1280 sc1\n"
"v_mfma_f32_16x16x32_bf16 v[52:55], v[156:159], v[124:127], v[52:55]\n"
"ds_read_b128 v[16:19], v213 offset:640\n"
"v_cvt_pk_bf16_f32 v37, v38, v39\n"
"v_mfma_f32_16x16x32_bf16 v[52:55], v[168:171], v[128:131], v[52:55]\n"
"buffer_store_dword v234, v225, s[24:27], s38 offen offset:1536 sc1\n"
"v_mfma_f32_16x16x32_bf16 v[56:59], v[172:175], v[128:131], v[56:59]\n"
"ds_read_b128 v[20:23], v213 offset:704\n"
"v_mfma_f32_16x16x32_bf16 v[56:59], v[160:163], v[124:127], v[56:59]\n"
"buffer_store_dword v235, v225, s[24:27], s38 offen offset:1792 sc1\n"
"v_mfma_f32_16x16x32_bf16 v[56:59], v[148:151], v[120:123], v[56:59]\n"
"ds_read_b128 v[24:27], v213 offset:768\n"
"v_mfma_f32_16x16x32_bf16 v[60:63], a[216:219], v[120:123], v[60:63]\n"
"buffer_store_dword v236, v225, s[24:27], s38 offen offset:2048 sc1\n"
"v_mfma_f32_16x16x32_bf16 v[60:63], a[228:231], v[124:127], v[60:63]\n"
"ds_read_b128 v[28:31], v213 offset:832\n"
"v_mfma_f32_16x16x32_bf16 v[60:63], a[240:243], v[128:131], v[60:63]\n"
"buffer_store_dword v237, v225, s[24:27], s38 offen offset:2304 sc1\n"
"v_mfma_f32_16x16x32_bf16 v[64:67], a[244:247], v[128:131], v[64:67]\n"
"ds_read_b128 v[32:35], v213 offset:896\n"
"v_mfma_f32_16x16x32_bf16 v[64:67], a[232:235], v[124:127], v[64:67]\n"
"buffer_store_dword v238, v225, s[24:27], s38 offen offset:2560 sc1\n"
"v_mfma_f32_16x16x32_bf16 v[64:67], a[220:223], v[120:123], v[64:67]\n"
"ds_write_b32 v219, v36 offset:0\n"
"v_mfma_f32_16x16x32_bf16 v[68:71], a[224:227], v[120:123], v[68:71]\n"
"buffer_store_dword v239, v225, s[24:27], s38 offen offset:2816 sc1\n"
"v_mfma_f32_16x16x32_bf16 v[68:71], a[236:239], v[124:127], v[68:71]\n"
"ds_write_b32 v219, v37 offset:4\n"
"v_mfma_f32_16x16x32_bf16 v[68:71], a[248:251], v[128:131], v[68:71]\n"
"ds_read_b128 v[120:123], v213 offset:768\n"
"s_waitcnt lgkmcnt(12)\n"
"v_mfma_f32_16x16x32_bf16 v[48:51], v[176:179], v[108:111], v[48:51]\n"
"buffer_store_dword v240, v225, s[24:27], s38 offen offset:3072 sc1\n"
"v_mfma_f32_16x16x32_bf16 v[48:51], v[188:191], v[112:115], v[48:51]\n"
"ds_read_b128 v[124:127], v213 offset:832\n"
"v_cvt_pk_bf16_f32 v40, v40, v41\n"
"v_mfma_f32_16x16x32_bf16 v[48:51], v[200:203], v[116:119], v[48:51]\n"
"buffer_store_dword v241, v225, s[24:27], s38 offen offset:3328 sc1\n"
"v_mfma_f32_16x16x32_bf16 v[52:55], v[204:207], v[116:119], v[52:55]\n"
"ds_read_b128 v[128:131], v213 offset:896\n"
"v_cvt_pk_bf16_f32 v41, v42, v43\n"
"v_mfma_f32_16x16x32_bf16 v[52:55], v[192:195], v[112:115], v[52:55]\n"
"buffer_store_dword v242, v225, s[24:27], s38 offen offset:3584 sc1\n"
"v_mfma_f32_16x16x32_bf16 v[52:55], v[180:183], v[108:111], v[52:55]\n"
"ds_write_b32 v219, v40 offset:32\n"
"v_mfma_f32_16x16x32_bf16 v[56:59], v[184:187], v[108:111], v[56:59]\n"
"buffer_store_dword v243, v225, s[24:27], s38 offen offset:3840 sc1\n"
"v_mfma_f32_16x16x32_bf16 v[56:59], v[196:199], v[112:115], v[56:59]\n"
"ds_write_b32 v219, v41 offset:36\n"
"v_mfma_f32_16x16x32_bf16 v[56:59], v[208:211], v[116:119], v[56:59]\n"
"buffer_store_dword v244, v226, s[24:27], s38 offen offset:0 sc1\n"
"v_mfma_f32_16x16x32_bf16 v[60:63], v[164:167], v[116:119], v[60:63]\n"
"v_cvt_pk_bf16_f32 v44, v44, v45\n"
"v_cvt_pk_bf16_f32 v45, v46, v47\n"
"v_mfma_f32_16x16x32_bf16 v[60:63], v[152:155], v[112:115], v[60:63]\n"
"buffer_store_dword v245, v226, s[24:27], s38 offen offset:256 sc1\n"
"v_mfma_f32_16x16x32_bf16 v[60:63], a[252:255], v[108:111], v[60:63]\n"
"ds_write_b32 v219, v44 offset:64\n"
"v_mfma_f32_16x16x32_bf16 v[64:67], v[144:147], v[108:111], v[64:67]\n"
"v_mfma_f32_16x16x32_bf16 v[64:67], v[156:159], v[112:115], v[64:67]\n"
"ds_write_b32 v219, v45 offset:68\n"
"v_mfma_f32_16x16x32_bf16 v[64:67], v[168:171], v[116:119], v[64:67]\n"
"v_cvt_pk_bf16_f32 v48, v48, v49\n"
"v_cvt_pk_bf16_f32 v49, v50, v51\n"
"v_mfma_f32_16x16x32_bf16 v[68:71], v[172:175], v[116:119], v[68:71]\n"
"ds_write_b32 v219, v48 offset:192\n"
"v_mfma_f32_16x16x32_bf16 v[68:71], v[160:163], v[112:115], v[68:71]\n"
"v_mfma_f32_16x16x32_bf16 v[68:71], v[148:151], v[108:111], v[68:71]\n"
"ds_read_b128 v[108:111], v212 offset:0\n"
"s_waitcnt lgkmcnt(6)\n"
"v_mfma_f32_16x16x32_bf16 v[60:63], v[176:179], v[120:123], v[60:63]\n"
"ds_read_b128 v[112:115], v212 offset:64\n"
"v_mfma_f32_16x16x32_bf16 v[60:63], v[188:191], v[124:127], v[60:63]\n"
"ds_read_b128 v[116:119], v212 offset:128\n"
"v_mfma_f32_16x16x32_bf16 v[60:63], v[200:203], v[128:131], v[60:63]\n"
"ds_write_b32 v219, v49 offset:196\n"
"v_mfma_f32_16x16x32_bf16 v[64:67], v[204:207], v[128:131], v[64:67]\n"
"v_cvt_pk_bf16_f32 v52, v52, v53\n"
"v_cvt_pk_bf16_f32 v53, v54, v55\n"
"v_mfma_f32_16x16x32_bf16 v[64:67], v[192:195], v[124:127], v[64:67]\n"
"ds_write_b32 v219, v52 offset:224\n"
"v_mfma_f32_16x16x32_bf16 v[64:67], v[180:183], v[120:123], v[64:67]\n"
"v_mfma_f32_16x16x32_bf16 v[68:71], v[184:187], v[120:123], v[68:71]\n"
"ds_write_b32 v219, v53 offset:228\n"
"v_mfma_f32_16x16x32_bf16 v[68:71], v[196:199], v[124:127], v[68:71]\n"
"v_cvt_pk_bf16_f32 v56, v56, v57\n"
"v_cvt_pk_bf16_f32 v57, v58, v59\n"
"v_mfma_f32_16x16x32_bf16 v[68:71], v[208:211], v[128:131], v[68:71]\n"
"ds_read_b128 v[120:123], v212 offset:192\n"
"s_waitcnt lgkmcnt(4)\n"
"v_mfma_f32_16x16x32_bf16 v[72:75], a[108:111], v[108:111], v[72:75]\n"
"ds_read_b128 v[124:127], v212 offset:256\n"
"v_mfma_f32_16x16x32_bf16 v[72:75], a[120:123], v[112:115], v[72:75]\n"
"ds_read_b128 v[128:131], v212 offset:320\n"
"v_mfma_f32_16x16x32_bf16 v[72:75], a[132:135], v[116:119], v[72:75]\n"
"ds_write_b32 v219, v56 offset:256\n"
"v_mfma_f32_16x16x32_bf16 v[76:79], a[136:139], v[116:119], v[76:79]\n"
"v_mfma_f32_16x16x32_bf16 v[76:79], a[124:127], v[112:115], v[76:79]\n"
"ds_write_b32 v219, v57 offset:260\n"
"v_mfma_f32_16x16x32_bf16 v[76:79], a[112:115], v[108:111], v[76:79]\n"
"v_cvt_pk_bf16_f32 v60, v60, v61\n"
"v_cvt_pk_bf16_f32 v61, v62, v63\n"
"v_mfma_f32_16x16x32_bf16 v[80:83], a[116:119], v[108:111], v[80:83]\n"
"ds_write_b32 v219, v60 offset:384\n"
"v_mfma_f32_16x16x32_bf16 v[80:83], a[128:131], v[112:115], v[80:83]\n"
"v_mfma_f32_16x16x32_bf16 v[80:83], a[140:143], v[116:119], v[80:83]\n"
"ds_read_b128 v[108:111], v212 offset:384\n"
"s_waitcnt lgkmcnt(4)\n"
"v_mfma_f32_16x16x32_bf16 v[72:75], a[144:147], v[120:123], v[72:75]\n"
"ds_read_b128 v[112:115], v212 offset:448\n"
"v_mfma_f32_16x16x32_bf16 v[72:75], a[156:159], v[124:127], v[72:75]\n"
"ds_read_b128 v[116:119], v212 offset:512\n"
"v_mfma_f32_16x16x32_bf16 v[72:75], a[168:171], v[128:131], v[72:75]\n"
"ds_write_b32 v219, v61 offset:388\n"
"v_mfma_f32_16x16x32_bf16 v[76:79], a[172:175], v[128:131], v[76:79]\n"
"v_cvt_pk_bf16_f32 v64, v64, v65\n"
"v_cvt_pk_bf16_f32 v65, v66, v67\n"
"v_mfma_f32_16x16x32_bf16 v[76:79], a[160:163], v[124:127], v[76:79]\n"
"ds_write_b32 v219, v64 offset:416\n"
"v_mfma_f32_16x16x32_bf16 v[76:79], a[148:151], v[120:123], v[76:79]\n"
"v_mfma_f32_16x16x32_bf16 v[80:83], a[152:155], v[120:123], v[80:83]\n"
"ds_write_b32 v219, v65 offset:420\n"
"v_mfma_f32_16x16x32_bf16 v[80:83], a[164:167], v[124:127], v[80:83]\n"
"v_cvt_pk_bf16_f32 v68, v68, v69\n"
"v_cvt_pk_bf16_f32 v69, v70, v71\n"
"v_mfma_f32_16x16x32_bf16 v[80:83], a[176:179], v[128:131], v[80:83]\n"
"ds_write_b32 v219, v68 offset:448\n"
"v_mfma_f32_16x16x32_bf16 v[84:87], a[132:135], v[128:131], v[84:87]\n"
"v_mfma_f32_16x16x32_bf16 v[84:87], a[120:123], v[124:127], v[84:87]\n"
"ds_write_b32 v219, v69 offset:452\n"
"v_mfma_f32_16x16x32_bf16 v[84:87], a[108:111], v[120:123], v[84:87]\n"
"v_mfma_f32_16x16x32_bf16 v[88:91], a[112:115], v[120:123], v[88:91]\n"
"v_mfma_f32_16x16x32_bf16 v[88:91], a[124:127], v[124:127], v[88:91]\n"
"v_mfma_f32_16x16x32_bf16 v[88:91], a[136:139], v[128:131], v[88:91]\n"
"v_mfma_f32_16x16x32_bf16 v[92:95], a[140:143], v[128:131], v[92:95]\n"
"v_mfma_f32_16x16x32_bf16 v[92:95], a[128:131], v[124:127], v[92:95]\n"
"s_add_u32 s38, s38, s37\n"
"s_waitcnt lgkmcnt(0)\n"
"s_barrier\n"
"v_mfma_f32_16x16x32_bf16 v[92:95], a[116:119], v[120:123], v[92:95]\n"
"ds_read_b128 v[120:123], v213 offset:576\n"
"v_mfma_f32_16x16x32_bf16 v[72:75], a[180:183], v[108:111], v[72:75]\n"
"ds_read_b128 v[124:127], v213 offset:640\n"
"v_mfma_f32_16x16x32_bf16 v[72:75], a[192:195], v[112:115], v[72:75]\n"
"ds_read_b128 v[128:131], v213 offset:704\n"
"v_mfma_f32_16x16x32_bf16 v[72:75], a[204:207], v[116:119], v[72:75]\n"
"ds_read_b32 v228, v220 offset:0\n"
"v_mfma_f32_16x16x32_bf16 v[76:79], a[208:211], v[116:119], v[76:79]\n"
"ds_read_b32 v229, v220 offset:256\n"
"v_mfma_f32_16x16x32_bf16 v[76:79], a[196:199], v[112:115], v[76:79]\n"
"ds_read_b32 v230, v221 offset:512\n"
"v_mfma_f32_16x16x32_bf16 v[76:79], a[184:187], v[108:111], v[76:79]\n"
"ds_read_b32 v231, v220 offset:784\n"
"v_mfma_f32_16x16x32_bf16 v[80:83], a[188:191], v[108:111], v[80:83]\n"
"ds_read_b32 v232, v222 offset:1040\n"
"v_mfma_f32_16x16x32_bf16 v[80:83], a[200:203], v[112:115], v[80:83]\n"
"ds_read_b32 v233, v220 offset:1312\n"
"v_mfma_f32_16x16x32_bf16 v[80:83], a[212:215], v[116:119], v[80:83]\n"
"ds_read_b32 v234, v223 offset:1568\n"
"v_mfma_f32_16x16x32_bf16 v[84:87], a[168:171], v[116:119], v[84:87]\n"
"ds_read_b32 v235, v220 offset:1840\n"
"v_mfma_f32_16x16x32_bf16 v[84:87], a[156:159], v[112:115], v[84:87]\n"
"ds_read_b32 v236, v220 offset:2096\n"
"v_mfma_f32_16x16x32_bf16 v[84:87], a[144:147], v[108:111], v[84:87]\n"
"ds_read_b32 v237, v220 offset:2368\n"
"v_mfma_f32_16x16x32_bf16 v[88:91], a[148:151], v[108:111], v[88:91]\n"
"ds_read_b32 v238, v220 offset:2624\n"
"v_mfma_f32_16x16x32_bf16 v[88:91], a[160:163], v[112:115], v[88:91]\n"
"ds_read_b32 v239, v221 offset:2880\n"
"v_mfma_f32_16x16x32_bf16 v[88:91], a[172:175], v[116:119], v[88:91]\n"
"ds_read_b32 v240, v220 offset:3152\n"
"v_mfma_f32_16x16x32_bf16 v[92:95], a[176:179], v[116:119], v[92:95]\n"
"ds_read_b32 v241, v222 offset:3408\n"
"v_mfma_f32_16x16x32_bf16 v[92:95], a[164:167], v[112:115], v[92:95]\n"
"ds_read_b32 v242, v220 offset:3680\n"
"v_mfma_f32_16x16x32_bf16 v[92:95], a[152:155], v[108:111], v[92:95]\n"
"ds_read_b128 v[108:111], v212 offset:20480\n"
"s_waitcnt lgkmcnt(15)\n"
"v_mfma_f32_16x16x32_bf16 v[84:87], a[180:183], v[120:123], v[84:87]\n"
"ds_read_b128 v[112:115], v212 offset:20544\n"
"v_mfma_f32_16x16x32_bf16 v[84:87], a[192:195], v[124:127], v[84:87]\n"
"ds_read_b128 v[116:119], v212 offset:20608\n"
"v_mfma_f32_16x16x32_bf16 v[84:87], a[204:207], v[128:131], v[84:87]\n"
"ds_read_b32 v243, v223 offset:3936\n"
"v_mfma_f32_16x16x32_bf16 v[88:91], a[208:211], v[128:131], v[88:91]\n"
"ds_read_b32 v244, v220 offset:4208\n"
"v_mfma_f32_16x16x32_bf16 v[88:91], a[196:199], v[124:127], v[88:91]\n"
"ds_read_b32 v245, v220 offset:4464\n"
"v_mfma_f32_16x16x32_bf16 v[88:91], a[184:187], v[120:123], v[88:91]\n"
"ds_read_b128 v[36:39], v212 offset:20864\n"
"v_mfma_f32_16x16x32_bf16 v[92:95], a[188:191], v[120:123], v[92:95]\n"
"ds_read_b128 v[40:43], v212 offset:20928\n"
"v_mfma_f32_16x16x32_bf16 v[92:95], a[200:203], v[124:127], v[92:95]\n"
"ds_read_b128 v[44:47], v212 offset:20992\n"
"v_mfma_f32_16x16x32_bf16 v[92:95], a[212:215], v[128:131], v[92:95]\n"
"ds_read_b128 v[120:123], v212 offset:20672\n"
"s_waitcnt lgkmcnt(7)\n"
"v_mfma_f32_16x16x32_bf16 v[72:75], a[216:219], v[108:111], v[72:75]\n"
"ds_read_b128 v[124:127], v212 offset:20736\n"
"s_waitcnt lgkmcnt(5)\n"
"v_mfma_f32_16x16x32_bf16 v[72:75], a[228:231], v[112:115], v[72:75]\n"
"buffer_store_dword v228, v225, s[24:27], s38 offen offset:0 sc1\n"
"v_mfma_f32_16x16x32_bf16 v[72:75], a[240:243], v[116:119], v[72:75]\n"
"ds_read_b128 v[128:131], v212 offset:20800\n"
"v_mfma_f32_16x16x32_bf16 v[76:79], a[244:247], v[116:119], v[76:79]\n"
"buffer_store_dword v229, v225, s[24:27], s38 offen offset:256 sc1\n"
"v_mfma_f32_16x16x32_bf16 v[76:79], a[232:235], v[112:115], v[76:79]\n"
"ds_read_b128 v[48:51], v213 offset:21056\n"
"v_mfma_f32_16x16x32_bf16 v[76:79], a[220:223], v[108:111], v[76:79]\n"
"buffer_store_dword v230, v225, s[24:27], s38 offen offset:512 sc1\n"
"v_mfma_f32_16x16x32_bf16 v[80:83], a[224:227], v[108:111], v[80:83]\n"
"ds_read_b128 v[52:55], v213 offset:21120\n"
"v_mfma_f32_16x16x32_bf16 v[80:83], a[236:239], v[112:115], v[80:83]\n"
"buffer_store_dword v231, v225, s[24:27], s38 offen offset:768 sc1\n"
"v_mfma_f32_16x16x32_bf16 v[80:83], a[248:251], v[116:119], v[80:83]\n"
"ds_read_b128 v[108:111], v212 offset:20864\n"
"s_waitcnt lgkmcnt(3)\n"
"v_mfma_f32_16x16x32_bf16 v[72:75], a[252:255], v[120:123], v[72:75]\n"
"buffer_store_dword v232, v225, s[24:27], s38 offen offset:1024 sc1\n"
"v_mfma_f32_16x16x32_bf16 v[72:75], v[152:155], v[124:127], v[72:75]\n"
"ds_read_b128 v[112:115], v212 offset:20928\n"
"v_mfma_f32_16x16x32_bf16 v[72:75], v[164:167], v[128:131], v[72:75]\n"
"buffer_store_dword v233, v225, s[24:27], s38 offen offset:1280 sc1\n"
"v_mfma_f32_16x16x32_bf16 v[76:79], v[168:171], v[128:131], v[76:79]\n"
"ds_read_b128 v[116:119], v212 offset:20992\n"
"v_mfma_f32_16x16x32_bf16 v[76:79], v[156:159], v[124:127], v[76:79]\n"
"buffer_store_dword v234, v225, s[24:27], s38 offen offset:1536 sc1\n"
"v_mfma_f32_16x16x32_bf16 v[76:79], v[144:147], v[120:123], v[76:79]\n"
"ds_read_b128 v[56:59], v213 offset:21184\n"
"v_mfma_f32_16x16x32_bf16 v[80:83], v[148:151], v[120:123], v[80:83]\n"
"buffer_store_dword v235, v225, s[24:27], s38 offen offset:1792 sc1\n"
"v_mfma_f32_16x16x32_bf16 v[80:83], v[160:163], v[124:127], v[80:83]\n"
"ds_read_b128 v[60:63], v213 offset:21248\n"
"v_mfma_f32_16x16x32_bf16 v[80:83], v[172:175], v[128:131], v[80:83]\n"
"buffer_store_dword v236, v225, s[24:27], s38 offen offset:2048 sc1\n"
"v_mfma_f32_16x16x32_bf16 v[84:87], a[240:243], v[128:131], v[84:87]\n"
"ds_read_b128 v[64:67], v213 offset:21312\n"
"v_mfma_f32_16x16x32_bf16 v[84:87], a[228:231], v[124:127], v[84:87]\n"
"buffer_store_dword v237, v225, s[24:27], s38 offen offset:2304 sc1\n"
"v_mfma_f32_16x16x32_bf16 v[84:87], a[216:219], v[120:123], v[84:87]\n"
"ds_read_b128 v[68:71], v213 offset:21376\n"
"v_mfma_f32_16x16x32_bf16 v[88:91], a[220:223], v[120:123], v[88:91]\n"
"buffer_store_dword v238, v225, s[24:27], s38 offen offset:2560 sc1\n"
"v_mfma_f32_16x16x32_bf16 v[88:91], a[232:235], v[124:127], v[88:91]\n"
"v_mfma_f32_16x16x32_bf16 v[88:91], a[244:247], v[128:131], v[88:91]\n"
"buffer_store_dword v239, v225, s[24:27], s38 offen offset:2816 sc1\n"
"v_mfma_f32_16x16x32_bf16 v[92:95], a[248:251], v[128:131], v[92:95]\n"
"v_mfma_f32_16x16x32_bf16 v[92:95], a[236:239], v[124:127], v[92:95]\n"
"buffer_store_dword v240, v225, s[24:27], s38 offen offset:3072 sc1\n"
"v_mfma_f32_16x16x32_bf16 v[92:95], a[224:227], v[120:123], v[92:95]\n"
"ds_read_b128 v[120:123], v213 offset:21056\n"
"s_waitcnt lgkmcnt(5)\n"
"v_mfma_f32_16x16x32_bf16 v[72:75], v[176:179], v[108:111], v[72:75]\n"
"buffer_store_dword v241, v225, s[24:27], s38 offen offset:3328 sc1\n"
"v_mfma_f32_16x16x32_bf16 v[72:75], v[188:191], v[112:115], v[72:75]\n"
"ds_read_b128 v[124:127], v213 offset:21120\n"
"v_mfma_f32_16x16x32_bf16 v[72:75], v[200:203], v[116:119], v[72:75]\n"
"buffer_store_dword v242, v225, s[24:27], s38 offen offset:3584 sc1\n"
"v_mfma_f32_16x16x32_bf16 v[76:79], v[204:207], v[116:119], v[76:79]\n"
"ds_read_b128 v[128:131], v213 offset:21184\n"
"v_mfma_f32_16x16x32_bf16 v[76:79], v[192:195], v[112:115], v[76:79]\n"
"buffer_store_dword v243, v225, s[24:27], s38 offen offset:3840 sc1\n"
"v_mfma_f32_16x16x32_bf16 v[76:79], v[180:183], v[108:111], v[76:79]\n"
"v_mfma_f32_16x16x32_bf16 v[80:83], v[184:187], v[108:111], v[80:83]\n"
"buffer_store_dword v244, v226, s[24:27], s38 offen offset:0 sc1\n"
"v_mfma_f32_16x16x32_bf16 v[80:83], v[196:199], v[112:115], v[80:83]\n"
"v_mfma_f32_16x16x32_bf16 v[80:83], v[208:211], v[116:119], v[80:83]\n"
"buffer_store_dword v245, v226, s[24:27], s38 offen offset:256 sc1\n"
"v_mfma_f32_16x16x32_bf16 v[84:87], v[164:167], v[116:119], v[84:87]\n"
"v_mfma_f32_16x16x32_bf16 v[84:87], v[152:155], v[112:115], v[84:87]\n"
"v_cvt_pk_bf16_f32 v72, v72, v73\n"
"v_cvt_pk_bf16_f32 v73, v74, v75\n"
"v_mfma_f32_16x16x32_bf16 v[84:87], a[252:255], v[108:111], v[84:87]\n"
"ds_write_b32 v219, v72 offset:59904\n"
"v_mfma_f32_16x16x32_bf16 v[88:91], v[144:147], v[108:111], v[88:91]\n"
"v_mfma_f32_16x16x32_bf16 v[88:91], v[156:159], v[112:115], v[88:91]\n"
"ds_write_b32 v219, v73 offset:59908\n"
"v_mfma_f32_16x16x32_bf16 v[88:91], v[168:171], v[116:119], v[88:91]\n"
"v_cvt_pk_bf16_f32 v76, v76, v77\n"
"v_cvt_pk_bf16_f32 v77, v78, v79\n"
"v_mfma_f32_16x16x32_bf16 v[92:95], v[172:175], v[116:119], v[92:95]\n"
"ds_write_b32 v219, v76 offset:59936\n"
"v_mfma_f32_16x16x32_bf16 v[92:95], v[160:163], v[112:115], v[92:95]\n"
"v_mfma_f32_16x16x32_bf16 v[92:95], v[148:151], v[108:111], v[92:95]\n"
"s_waitcnt lgkmcnt(3)\n"
"v_mfma_f32_16x16x32_bf16 v[84:87], v[176:179], v[120:123], v[84:87]\n"
"ds_write_b32 v219, v77 offset:59940\n"
"v_mfma_f32_16x16x32_bf16 v[84:87], v[188:191], v[124:127], v[84:87]\n"
"v_cvt_pk_bf16_f32 v80, v80, v81\n"
"v_cvt_pk_bf16_f32 v81, v82, v83\n"
"v_mfma_f32_16x16x32_bf16 v[84:87], v[200:203], v[128:131], v[84:87]\n"
"ds_write_b32 v219, v80 offset:59968\n"
"v_mfma_f32_16x16x32_bf16 v[88:91], v[204:207], v[128:131], v[88:91]\n"
"v_mfma_f32_16x16x32_bf16 v[88:91], v[192:195], v[124:127], v[88:91]\n"
"ds_write_b32 v219, v81 offset:59972\n"
"v_mfma_f32_16x16x32_bf16 v[88:91], v[180:183], v[120:123], v[88:91]\n"
"v_mfma_f32_16x16x32_bf16 v[92:95], v[184:187], v[120:123], v[92:95]\n"
"v_mfma_f32_16x16x32_bf16 v[92:95], v[196:199], v[124:127], v[92:95]\n"
"v_mfma_f32_16x16x32_bf16 v[92:95], v[208:211], v[128:131], v[92:95]\n"
"v_mfma_f32_16x16x32_bf16 v[96:99], a[108:111], v[0:3], v[96:99]\n"
"v_mfma_f32_16x16x32_bf16 v[96:99], a[120:123], v[4:7], v[96:99]\n"
"v_cvt_pk_bf16_f32 v84, v84, v85\n"
"v_cvt_pk_bf16_f32 v85, v86, v87\n"
"v_mfma_f32_16x16x32_bf16 v[96:99], a[132:135], v[8:11], v[96:99]\n"
"ds_write_b32 v219, v84 offset:60096\n"
"v_mfma_f32_16x16x32_bf16 v[96:99], a[144:147], v[12:15], v[96:99]\n"
"v_mfma_f32_16x16x32_bf16 v[96:99], a[156:159], v[16:19], v[96:99]\n"
"ds_write_b32 v219, v85 offset:60100\n"
"v_mfma_f32_16x16x32_bf16 v[96:99], a[168:171], v[20:23], v[96:99]\n"
"v_cvt_pk_bf16_f32 v88, v88, v89\n"
"v_cvt_pk_bf16_f32 v89, v90, v91\n"
"v_mfma_f32_16x16x32_bf16 v[96:99], a[180:183], v[24:27], v[96:99]\n"
"ds_write_b32 v219, v88 offset:60128\n"
"v_mfma_f32_16x16x32_bf16 v[96:99], a[192:195], v[28:31], v[96:99]\n"
"v_mfma_f32_16x16x32_bf16 v[96:99], a[204:207], v[32:35], v[96:99]\n"
"ds_write_b32 v219, v89 offset:60132\n"
"v_mfma_f32_16x16x32_bf16 v[96:99], a[216:219], v[36:39], v[96:99]\n"
"v_cvt_pk_bf16_f32 v92, v92, v93\n"
"v_cvt_pk_bf16_f32 v93, v94, v95\n"
"v_mfma_f32_16x16x32_bf16 v[96:99], a[228:231], v[40:43], v[96:99]\n"
"ds_write_b32 v219, v92 offset:60160\n"
"v_mfma_f32_16x16x32_bf16 v[96:99], a[240:243], v[44:47], v[96:99]\n"
"v_mfma_f32_16x16x32_bf16 v[96:99], a[252:255], v[48:51], v[96:99]\n"
"ds_write_b32 v219, v93 offset:60164\n"
"v_mfma_f32_16x16x32_bf16 v[96:99], v[152:155], v[52:55], v[96:99]\n"
"v_mfma_f32_16x16x32_bf16 v[96:99], v[164:167], v[56:59], v[96:99]\n"
"v_mfma_f32_16x16x32_bf16 v[96:99], v[176:179], v[60:63], v[96:99]\n"
"v_mfma_f32_16x16x32_bf16 v[96:99], v[188:191], v[64:67], v[96:99]\n"
"v_mfma_f32_16x16x32_bf16 v[96:99], v[200:203], v[68:71], v[96:99]\n"
"v_mfma_f32_16x16x32_bf16 v[100:103], v[204:207], v[68:71], v[100:103]\n"
"s_add_u32 s38, s38, s37\n"
"s_waitcnt lgkmcnt(0)\n"
"s_barrier\n"
"v_mfma_f32_16x16x32_bf16 v[100:103], v[192:195], v[64:67], v[100:103]\n"
"ds_read_b128 v[228:231], v248 offset:59904\n"
"v_mfma_f32_16x16x32_bf16 v[100:103], v[180:183], v[60:63], v[100:103]\n"
"ds_read_b128 v[232:235], v248 offset:62272\n"
"v_mfma_f32_16x16x32_bf16 v[100:103], v[168:171], v[56:59], v[100:103]\n"
"ds_read_b128 v[236:239], v249 offset:59904\n"
"v_mfma_f32_16x16x32_bf16 v[100:103], v[156:159], v[52:55], v[100:103]\n"
"v_mfma_f32_16x16x32_bf16 v[100:103], v[144:147], v[48:51], v[100:103]\n"
"v_mfma_f32_16x16x32_bf16 v[100:103], a[244:247], v[44:47], v[100:103]\n"
"v_mfma_f32_16x16x32_bf16 v[100:103], a[232:235], v[40:43], v[100:103]\n"
"v_cvt_pk_bf16_f32 v96, v96, v97\n"
"v_cvt_pk_bf16_f32 v97, v98, v99\n"
"v_mfma_f32_16x16x32_bf16 v[100:103], a[220:223], v[36:39], v[100:103]\n"
"ds_write_b32 v219, v96 offset:60288\n"
"v_mfma_f32_16x16x32_bf16 v[100:103], a[208:211], v[32:35], v[100:103]\n"
"s_waitcnt lgkmcnt(3)\n"
"v_mfma_f32_16x16x32_bf16 v[100:103], a[196:199], v[28:31], v[100:103]\n"
"buffer_store_dwordx4 v[228:231], v214, s[24:27], s38 offen offset:0 sc1\n"
"v_mfma_f32_16x16x32_bf16 v[100:103], a[184:187], v[24:27], v[100:103]\n"
"ds_write_b32 v219, v97 offset:60292\n"
"v_mfma_f32_16x16x32_bf16 v[100:103], a[172:175], v[20:23], v[100:103]\n"
"v_mfma_f32_16x16x32_bf16 v[100:103], a[160:163], v[16:19], v[100:103]\n"
"s_waitcnt lgkmcnt(3)\n"
"v_mfma_f32_16x16x32_bf16 v[100:103], a[148:151], v[12:15], v[100:103]\n"
"buffer_store_dwordx4 v[232:235], v214, s[24:27], s38 offen offset:2304 sc1\n"
"v_mfma_f32_16x16x32_bf16 v[100:103], a[136:139], v[8:11], v[100:103]\n"
"v_mfma_f32_16x16x32_bf16 v[100:103], a[124:127], v[4:7], v[100:103]\n"
"v_mfma_f32_16x16x32_bf16 v[100:103], a[112:115], v[0:3], v[100:103]\n"
"s_waitcnt lgkmcnt(2)\n"
"v_mfma_f32_16x16x32_bf16 v[104:107], a[116:119], v[0:3], v[104:107]\n"
"buffer_store_dwordx4 v[236:239], v215, s[24:27], s38 offen offset:0 sc1\n"
"v_mfma_f32_16x16x32_bf16 v[104:107], a[128:131], v[4:7], v[104:107]\n"
"v_mfma_f32_16x16x32_bf16 v[104:107], a[140:143], v[8:11], v[104:107]\n"
"v_mfma_f32_16x16x32_bf16 v[104:107], a[152:155], v[12:15], v[104:107]\n"
"v_mfma_f32_16x16x32_bf16 v[104:107], a[164:167], v[16:19], v[104:107]\n"
"v_mfma_f32_16x16x32_bf16 v[104:107], a[176:179], v[20:23], v[104:107]\n"
"v_mfma_f32_16x16x32_bf16 v[104:107], a[188:191], v[24:27], v[104:107]\n"
"v_mfma_f32_16x16x32_bf16 v[104:107], a[200:203], v[28:31], v[104:107]\n"
"v_cvt_pk_bf16_f32 v100, v100, v101\n"
"v_cvt_pk_bf16_f32 v101, v102, v103\n"
"v_mfma_f32_16x16x32_bf16 v[104:107], a[212:215], v[32:35], v[104:107]\n"
"ds_write_b32 v219, v100 offset:60320\n"
"v_mfma_f32_16x16x32_bf16 v[104:107], a[224:227], v[36:39], v[104:107]\n"
"v_mfma_f32_16x16x32_bf16 v[104:107], a[236:239], v[40:43], v[104:107]\n"
"ds_write_b32 v219, v101 offset:60324\n"
"v_mfma_f32_16x16x32_bf16 v[104:107], a[248:251], v[44:47], v[104:107]\n"
"v_mfma_f32_16x16x32_bf16 v[104:107], v[148:151], v[48:51], v[104:107]\n"
"v_mfma_f32_16x16x32_bf16 v[104:107], v[160:163], v[52:55], v[104:107]\n"
"v_mfma_f32_16x16x32_bf16 v[104:107], v[172:175], v[56:59], v[104:107]\n"
"v_mfma_f32_16x16x32_bf16 v[104:107], v[184:187], v[60:63], v[104:107]\n"
"v_mfma_f32_16x16x32_bf16 v[104:107], v[196:199], v[64:67], v[104:107]\n"
"v_mfma_f32_16x16x32_bf16 v[104:107], v[208:211], v[68:71], v[104:107]\n"
"s_nop 7\n"
"v_cvt_pk_bf16_f32 v104, v104, v105\n"
"v_cvt_pk_bf16_f32 v105, v106, v107\n"
"ds_write_b64 v219, v[104:105] offset:60352\n"
"s_waitcnt lgkmcnt(0)\n"
"s_barrier\n"
"ds_read_b128 v[240:243], v250 offset:59904\n"
"ds_read_b128 v[244:247], v250 offset:62272\n"
"s_waitcnt lgkmcnt(1)\n"
"buffer_store_dwordx4 v[240:243], v216, s[24:27], s38 offen offset:0 sc1\n"
"s_waitcnt lgkmcnt(0)\n"
"buffer_store_dwordx4 v[244:247], v216, s[24:27], s38 offen offset:2304 sc1\n"
".Lconv_end:\n"
  :: "s"(ka), "s"(wave), "s"(bid) : "v0","v1","v2","v3","v4","v5","v6","v7","v8","v9","v10","v11","v12","v13","v14","v15","v16","v17","v18","v19","v20","v21","v22","v23","v24","v25","v26","v27","v28","v29","v30","v31","v32","v33","v34","v35","v36","v37","v38","v39","v40","v41","v42","v43","v44","v45","v46","v47","v48","v49","v50","v51","v52","v53","v54","v55","v56","v57","v58","v59","v60","v61","v62","v63","v64","v65","v66","v67","v68","v69","v70","v71","v72","v73","v74","v75","v76","v77","v78","v79","v80","v81","v82","v83","v84","v85","v86","v87","v88","v89","v90","v91","v92","v93","v94","v95","v96","v97","v98","v99","v100","v101","v102","v103","v104","v105","v106","v107","v108","v109","v110","v111","v112","v113","v114","v115","v116","v117","v118","v119","v120","v121","v122","v123","v124","v125","v126","v127","v128","v129","v130","v131","v132","v133","v134","v135","v136","v137","v138","v139","v140","v141","v142","v143","v144","v145","v146","v147","v148","v149","v150","v151","v152","v153","v154","v155","v156","v157","v158","v159","v160","v161","v162","v163","v164","v165","v166","v167","v168","v169","v170","v171","v172","v173","v174","v175","v176","v177","v178","v179","v180","v181","v182","v183","v184","v185","v186","v187","v188","v189","v190","v191","v192","v193","v194","v195","v196","v197","v198","v199","v200","v201","v202","v203","v204","v205","v206","v207","v208","v209","v210","v211","v212","v213","v214","v215","v216","v217","v218","v219","v220","v221","v222","v223","v224","v225","v226","v227","v228","v229","v230","v231","v232","v233","v234","v235","v236","v237","v238","v239","v240","v241","v242","v243","v244","v245","v246","v247","v248","v249","v250","v251","v252","v253","v254","v255","a0","a1","a2","a3","a4","a5","a6","a7","a8","a9","a10","a11","a12","a13","a14","a15","a16","a17","a18","a19","a20","a21","a22","a23","a24","a25","a26","a27","a28","a29","a30","a31","a32","a33","a34","a35","a36","a37","a38","a39","a40","a41","a42","a43","a44","a45","a46","a47","a48","a49","a50","a51","a52","a53","a54","a55","a56","a57","a58","a59","a60","a61","a62","a63","a64","a65","a66","a67","a68","a69","a70","a71","a72","a73","a74","a75","a76","a77","a78","a79","a80","a81","a82","a83","a84","a85","a86","a87","a88","a89","a90","a91","a92","a93","a94","a95","a96","a97","a98","a99","a100","a101","a102","a103","a104","a105","a106","a107","a108","a109","a110","a111","a112","a113","a114","a115","a116","a117","a118","a119","a120","a121","a122","a123","a124","a125","a126","a127","a128","a129","a130","a131","a132","a133","a134","a135","a136","a137","a138","a139","a140","a141","a142","a143","a144","a145","a146","a147","a148","a149","a150","a151","a152","a153","a154","a155","a156","a157","a158","a159","a160","a161","a162","a163","a164","a165","a166","a167","a168","a169","a170","a171","a172","a173","a174","a175","a176","a177","a178","a179","a180","a181","a182","a183","a184","a185","a186","a187","a188","a189","a190","a191","a192","a193","a194","a195","a196","a197","a198","a199","a200","a201","a202","a203","a204","a205","a206","a207","a208","a209","a210","a211","a212","a213","a214","a215","a216","a217","a218","a219","a220","a221","a222","a223","a224","a225","a226","a227","a228","a229","a230","a231","a232","a233","a234","a235","a236","a237","a238","a239","a240","a241","a242","a243","a244","a245","a246","a247","a248","a249","a250","a251","a252","a253","a254","a255","s4","s5","s6","s7","s8","s9","s10","s11","s12","s13","s14","s15","s16","s17","s18","s19","s20","s21","s22","s23","s24","s25","s26","s27","s28","s29","s30","s31","s36","s37","s38","s39","s40","s41","s42","s43","s44","s45","s46","s47","s48","s49","s50","s51","s52","s53","s54","s55","s56","s57","s58","s59","s60","s61","s62","s63","s64","s65","s66","s67","s68","s69","s70","s71","s72","s73","s74","s75","s76","s77","s78","s79","s80","s81","s82","s83","s84","s85","s86","s87","s88","s89","s90","s91","s92","s93","s94","s95","s96","s97","s98","s99","vcc","scc","memory");
  asm volatile("" :: "s"((__attribute__((address_space(3))) char*)lds));
}
extern "C" void kernel_launch(void* const* d_in, const int* in_sizes, int n_in, void* d_out, int out_size,
                              void* d_ws, size_t ws_size, hipStream_t stream) {
  conv3x3_kernel<<<256, 256, 0, stream>>>(d_in[0], d_in[1], d_in[2], d_out);
}
```
